```python
import torch

def _softmax(x, axis=-1):
    return torch.softmax(x, dim=axis)
def _gelu(x):
    return torch.nn.functional.gelu(x, approximate="tanh")
def _silu(x):
    return torch.nn.functional.silu(x)
def _sigmoid(x):
    return torch.sigmoid(x)
def _layernorm(x, axis=-1, eps=1e-5):
    m = x.mean(dim=axis, keepdim=True)
    v = x.var(dim=axis, keepdim=True, unbiased=False)
    return (x - m) / torch.sqrt(v + eps)
def _scatter_add(tgt, idx, src, axis=0):
    return tgt.clone().index_add_(axis, idx, src)
def _segment_sum(src, seg, n):
    out = torch.zeros((n,) + src.shape[1:], dtype=src.dtype)
    return out.index_add_(0, seg, src)
def _pad(x, pw):
    flat = tuple(v for pair in reversed(pw) for v in pair)
    return torch.nn.functional.pad(x, flat)

kernel_name = 'parallel_sum__mha_block_cached__swiglu_ffn__decode'

def setup_inputs():

    _w = (lambda a: (lambda t: t.to(torch.bfloat16) if t.is_floating_point() else t)(torch.as_tensor(a)))
    def _A_setup():
        B, Sq, D = 2, 1, 448
        H, Sk, Dh = 16, 8192, 256
        return {
            "x": _w((lambda *s: torch.randn(*s, dtype=torch.bfloat16) * 0.05)(B, Sq, D)),
            "k_cache": _w((lambda *s: torch.randn(*s, dtype=torch.bfloat16) * 3.2)(B, H, Sk, Dh)),
            "v_cache": _w((lambda *s: torch.randn(*s, dtype=torch.bfloat16) * 1.6)(B, H, Sk, Dh)),
            "wq": _w((lambda *s: torch.randn(*s, dtype=torch.bfloat16) * 3.2)(D, H * Dh) * (D ** -0.5)),
            "wo": _w((lambda *s: torch.randn(*s, dtype=torch.bfloat16) * 1.6)(H * Dh, D) * ((H * Dh) ** -0.5)),
        }
    _A_in = _A_setup()

    def _B_setup():
        B, S, D, F = 2, 1, 448, 1792
        return {
            "x": _w((lambda *s: torch.randn(*s, dtype=torch.bfloat16) * 0.05)(B, S, D)),
            "w_gate": _w((lambda *s: torch.randn(*s, dtype=torch.bfloat16) * 0.05)(D, F)),
            "w_up": _w((lambda *s: torch.randn(*s, dtype=torch.bfloat16) * 0.05)(D, F)),
            "w_down": _w((lambda *s: torch.randn(*s, dtype=torch.bfloat16) * 0.05)(F, D)),
        }
    _B_in = _B_setup()

    return {
        "A_x": _A_in["x"],
        "A_k_cache": _A_in["k_cache"],
        "A_v_cache": _A_in["v_cache"],
        "A_wq": _A_in["wq"],
        "A_wo": _A_in["wo"],
        "B_x": _B_in["x"],
        "B_w_gate": _B_in["w_gate"],
        "B_w_up": _B_in["w_up"],
        "B_w_down": _B_in["w_down"],
    }

def reference(A_x, A_k_cache, A_v_cache, A_wq, A_wo, B_x, B_w_gate, B_w_up, B_w_down):
    def _A_ref(x, k_cache, v_cache, wq, wo):
        B, Sq, D = x.shape
        H, Dh = 16, 256
        q = (lambda x, *d: x.permute(*d))((x @ wq).reshape(B, Sq, H, Dh), 0, 2, 1, 3)
        scores = torch.einsum("bhqd,bhkd->bhqk", q, k_cache) * (Dh ** -0.5)
        p = _softmax(scores, axis=-1)
        o = torch.einsum("bhqk,bhkd->bhqd", p, v_cache)
        return (lambda x, *d: x.permute(*d))(o, 0, 2, 1, 3).reshape(B, Sq, H * Dh) @ wo

    def _B_ref(x, w_gate, w_up, w_down):
        g = _silu(x @ w_gate)
        u = x @ w_up
        return (g * u) @ w_down

    x = A_x
    xn = _layernorm(x, axis=-1)
    return x + _A_ref(xn, A_k_cache, A_v_cache, A_wq, A_wo) + _B_ref(xn, B_w_gate, B_w_up, B_w_down)
```

```cpp
#include <hip/hip_runtime.h>
#include <hip/hip_bf16.h>
#include <stdint.h>

typedef unsigned int u32;
typedef __attribute__((ext_vector_type(4))) unsigned u32x4;
typedef __attribute__((ext_vector_type(2))) __bf16 bf16x2;

constexpr int B = 2, D = 448, H = 16, SK = 8192, DH = 256, F = 1792;
constexpr int NSPLIT = 8;
constexpr int KEYS = SK / NSPLIT;

__device__ __forceinline__ float bf2f(unsigned short v) { return __uint_as_float(((u32)v) << 16); }
__device__ __forceinline__ float bflo(u32 v) { return __uint_as_float(v << 16); }
__device__ __forceinline__ float bfhi(u32 v) { return __uint_as_float(v & 0xffff0000u); }
__device__ __forceinline__ float rbf(float f) { return __bfloat162float(__float2bfloat16(f)); }

constexpr int WS_XN = 0, WS_Q = 1024, WS_H = WS_Q + 8192, WS_P = WS_H + 4096;

__device__ void layernorm(const unsigned short* x, float* xn_lds, float* red) {
  int t = threadIdx.x;
  for (int b = 0; b < B; ++b) {
    float v0 = t < D ? bf2f(x[b * D + t]) : 0.f;
    float v1 = t + 256 < D ? bf2f(x[b * D + t + 256]) : 0.f;
    float s = v0 + v1;
    for (int o = 32; o; o >>= 1) s += __shfl_xor(s, o);
    if ((t & 63) == 0) red[t >> 6] = s;
    __syncthreads();
    float m = (red[0] + red[1] + red[2] + red[3]) / D;
    __syncthreads();
    float d0 = t < D ? v0 - m : 0.f, d1 = t + 256 < D ? v1 - m : 0.f;
    float q = d0 * d0 + d1 * d1;
    for (int o = 32; o; o >>= 1) q += __shfl_xor(q, o);
    if ((t & 63) == 0) red[t >> 6] = q;
    __syncthreads();
    float var = (red[0] + red[1] + red[2] + red[3]) / D;
    __syncthreads();
    float r = 1.f / sqrtf(var + 1e-5f);
    if (t < D) xn_lds[b * D + t] = rbf(d0 * r);
    if (t + 256 < D) xn_lds[b * D + t + 256] = rbf(d1 * r);
  }
  __syncthreads();
}

__global__ void k1(const unsigned short* x, const unsigned short* wq, const unsigned short* wg, const unsigned short* wu, float* ws) {
  __shared__ float xn[B * D];
  __shared__ float red[4];
  __shared__ float part[4][64][4];
  layernorm(x, xn, red);
  int t = threadIdx.x, c = t & 63, g = t >> 6;
  int blk = blockIdx.x;
  if (blk < 64) {
    int col = blk * 64 + c;
    float a0 = 0, a1 = 0;
    for (int r = g; r < D; r += 4) { float w = bf2f(wq[(size_t)r * 4096 + col]); a0 += xn[r] * w; a1 += xn[D + r] * w; }
    part[g][c][0] = a0; part[g][c][1] = a1;
    __syncthreads();
    if (g == 0) {
      a0 = part[0][c][0] + part[1][c][0] + part[2][c][0] + part[3][c][0];
      a1 = part[0][c][1] + part[1][c][1] + part[2][c][1] + part[3][c][1];
      ws[WS_Q + col] = rbf(a0); ws[WS_Q + 4096 + col] = rbf(a1);
    }
  } else {
    int col = (blk - 64) * 64 + c;
    float g0 = 0, g1 = 0, u0 = 0, u1 = 0;
    for (int r = g; r < D; r += 4) {
      float w = bf2f(wg[(size_t)r * F + col]); g0 += xn[r] * w; g1 += xn[D + r] * w;
      float v = bf2f(wu[(size_t)r * F + col]); u0 += xn[r] * v; u1 += xn[D + r] * v;
    }
    part[g][c][0] = g0; part[g][c][1] = g1; part[g][c][2] = u0; part[g][c][3] = u1;
    __syncthreads();
    if (g == 0) {
      float s[4];
      for (int i = 0; i < 4; ++i) s[i] = part[0][c][i] + part[1][c][i] + part[2][c][i] + part[3][c][i];
      float h0 = s[0] / (1.f + __expf(-s[0])) * s[2];
      float h1 = s[1] / (1.f + __expf(-s[1])) * s[3];
      ws[WS_H + col] = h0; ws[WS_H + F + col] = h1;
    }
  }
}

__global__ void __launch_bounds__(512) k2(const u32x4* K, const u32x4* V, float* ws) {
  __shared__ float sc[KEYS];
  __shared__ float red[16];
  __shared__ float accs[8][2][256];
  int blk = blockIdx.x, bh = blk / NSPLIT, sp = blk % NSPLIT;
  int t = threadIdx.x, lane = t & 63, w = t >> 6, half = lane >> 5, l32 = lane & 31;
  float q[8];
  for (int i = 0; i < 8; ++i) q[i] = ws[WS_Q + bh * 256 + l32 * 8 + i];
  size_t base = ((size_t)bh * SK + sp * KEYS) * 32;
  for (int i = 0; i < 64; ++i) {
    int key = (i * 8 + w) * 2 + half;
    u32x4 k = __builtin_nontemporal_load(&K[base + (size_t)key * 32 + l32]);
    float s = q[0] * bflo(k[0]) + q[1] * bfhi(k[0]) + q[2] * bflo(k[1]) + q[3] * bfhi(k[1]) + q[4] * bflo(k[2]) + q[5] * bfhi(k[2]) + q[6] * bflo(k[3]) + q[7] * bfhi(k[3]);
    for (int o = 16; o; o >>= 1) s += __shfl_xor(s, o);
    if (l32 == 0) sc[key] = s * 0.0625f;
  }
  __syncthreads();
  float m = fmaxf(sc[t], sc[t + 512]);
  for (int o = 32; o; o >>= 1) m = fmaxf(m, __shfl_xor(m, o));
  if (lane == 0) red[w] = m;
  __syncthreads();
  m = red[0];
  for (int i = 1; i < 8; ++i) m = fmaxf(m, red[i]);
  float p0 = __expf(sc[t] - m), p1 = __expf(sc[t + 512] - m);
  sc[t] = p0; sc[t + 512] = p1;
  float l = p0 + p1;
  for (int o = 32; o; o >>= 1) l += __shfl_xor(l, o);
  if (lane == 0) red[8 + w] = l;
  __syncthreads();
  l = 0;
  for (int i = 0; i < 8; ++i) l += red[8 + i];
  float acc[8] = {0, 0, 0, 0, 0, 0, 0, 0};
  for (int i = 0; i < 64; ++i) {
    int key = (i * 8 + w) * 2 + half;
    u32x4 v = __builtin_nontemporal_load(&V[base + (size_t)key * 32 + l32]);
    float p = sc[key];
    acc[0] += p * bflo(v[0]); acc[1] += p * bfhi(v[0]); acc[2] += p * bflo(v[1]); acc[3] += p * bfhi(v[1]);
    acc[4] += p * bflo(v[2]); acc[5] += p * bfhi(v[2]); acc[6] += p * bflo(v[3]); acc[7] += p * bfhi(v[3]);
  }
  for (int i = 0; i < 8; ++i) accs[w][half][l32 * 8 + i] = acc[i];
  __syncthreads();
  if (t < 256) {
    float s = 0;
    for (int i = 0; i < 8; ++i) s += accs[i][0][t] + accs[i][1][t];
    ws[WS_P + blk * 260 + t] = s;
    if (t == 0) { ws[WS_P + blk * 260 + 256] = m; ws[WS_P + blk * 260 + 257] = l; }
  }
}

__global__ void k3(const unsigned short* x, const unsigned short* wo, const unsigned short* wd, const float* ws, __hip_bfloat16* out) {
  __shared__ float o[2][4096];
  __shared__ float part[32][8][2];
  int t = threadIdx.x;
  for (int idx = t; idx < 8192; idx += 256) {
    int bh = idx >> 8, d = idx & 255;
    float M = -INFINITY;
    for (int s = 0; s < NSPLIT; ++s) M = fmaxf(M, ws[WS_P + (bh * NSPLIT + s) * 260 + 256]);
    float L = 0, a = 0;
    for (int s = 0; s < NSPLIT; ++s) {
      const float* p = ws + WS_P + (bh * NSPLIT + s) * 260;
      float wgt = __expf(p[256] - M);
      L += wgt * p[257]; a += wgt * p[d];
    }
    o[bh >> 4][(bh & 15) * 256 + d] = rbf(a / L);
  }
  __syncthreads();
  int c = t & 7, g = t >> 3;
  int col = blockIdx.x * 8 + c;
  float a0 = 0, a1 = 0, f0 = 0, f1 = 0;
  for (int r = g; r < 4096; r += 32) { float w = bf2f(wo[(size_t)r * D + col]); a0 += o[0][r] * w; a1 += o[1][r] * w; }
  for (int r = g; r < F; r += 32) { float w = bf2f(wd[(size_t)r * D + col]); f0 += ws[WS_H + r] * w; f1 += ws[WS_H + F + r] * w; }
  part[g][c][0] = a0 + f0; part[g][c][1] = a1 + f1;
  __syncthreads();
  if (t < 16) {
    int b = t >> 3; c = t & 7; col = blockIdx.x * 8 + c;
    float s = 0;
    for (int i = 0; i < 32; ++i) s += part[i][c][b];
    out[b * D + col] = __float2bfloat16(bf2f(x[b * D + col]) + s);
  }
}

extern "C" void kernel_launch(void* const* d_in, const int* in_sizes, int n_in,
                              void* d_out, int out_size, void* d_ws, size_t ws_size,
                              hipStream_t stream) {
  const unsigned short* x = (const unsigned short*)d_in[0];
  const u32x4* K = (const u32x4*)d_in[1];
  const u32x4* V = (const u32x4*)d_in[2];
  const unsigned short* wq = (const unsigned short*)d_in[3];
  const unsigned short* wo = (const unsigned short*)d_in[4];
  const unsigned short* wg = (const unsigned short*)d_in[6];
  const unsigned short* wu = (const unsigned short*)d_in[7];
  const unsigned short* wd = (const unsigned short*)d_in[8];
  float* ws = (float*)d_ws;
  k1<<<64 + 28, 256, 0, stream>>>(x, wq, wg, wu, ws);
  k2<<<256, 512, 0, stream>>>(K, V, ws);
  k3<<<56, 256, 0, stream>>>(x, wo, wd, ws, (__hip_bfloat16*)d_out);
}
```

```cpp
#include <hip/hip_runtime.h>
#include <hip/hip_bf16.h>
#include <stdint.h>

#define KMODE 0
#define VMODE 0
#define MAPX 0
#define INFL 3
#define WONT 1
#define WOB 4
#define QDELAY 16
#define POLLSLEEP 8

typedef unsigned int u32;
typedef unsigned short u16;
typedef unsigned long long u64;
typedef __attribute__((ext_vector_type(4))) unsigned u32x4;
typedef __attribute__((ext_vector_type(4))) float f32x4;
typedef __attribute__((ext_vector_type(2))) float f32x2;
typedef __attribute__((ext_vector_type(2))) __bf16 bf16x2;
#define GAS __attribute__((address_space(1)))
typedef GAS u32 gu32;
typedef GAS u64 gu64;
#define RLX_AGENT __ATOMIC_RELAXED, __HIP_MEMORY_SCOPE_AGENT

constexpr int D = 448, H = 16, SK = 8192, DH = 256, F = 1792;
constexpr int NWG = 256, NT = 640, NW = 8, RING = 16;
constexpr int NLD = 64;
constexpr unsigned SPIN_CAP = 1u << 18;

constexpr int G_E = 0;
constexpr int G_Q = 32;
constexpr int G_ST = G_Q + 16 * 2 * 4 * 256;
constexpr int G_S = G_ST + 2 * 32 * 16;
constexpr int G_F = G_S + 2 * 256 * 256;
constexpr int G_Y = G_F + 28 * 2 * 8 * 128;

__device__ __forceinline__ float bflo(u32 v) { return __uint_as_float(v << 16); }
__device__ __forceinline__ float bfhi(u32 v) { return __uint_as_float(v & 0xffff0000u); }
__device__ __forceinline__ u32 cvtpk(float lo, float hi) {
  u32 r; asm("v_cvt_pk_bf16_f32 %0, %1, %2" : "=v"(r) : "v"(lo), "v"(hi)); return r;
}
__device__ __forceinline__ float rbf(float f) { return bflo(cvtpk(f, f)); }
__device__ __forceinline__ float dot2(u32 a, u32 b, float c) {
  return __builtin_amdgcn_fdot2_f32_bf16(__builtin_bit_cast(bf16x2, a), __builtin_bit_cast(bf16x2, b), c, false);
}
template <int CTRL> __device__ __forceinline__ float dpp(float x) {
  return __builtin_bit_cast(float, __builtin_amdgcn_mov_dpp(__builtin_bit_cast(int, x), CTRL, 0xf, 0xf, true));
}
constexpr int XOR1 = 0xB1, XOR2 = 0x4E, HMIR = 0x141, MIR = 0x140, ROR8 = 0x128;
__device__ __forceinline__ float sum16(float x) {
  x += dpp<XOR1>(x); x += dpp<XOR2>(x); x += dpp<HMIR>(x); x += dpp<MIR>(x); return x;
}
__device__ __forceinline__ float max16(float x) {
  x = fmaxf(x, dpp<XOR1>(x)); x = fmaxf(x, dpp<XOR2>(x)); x = fmaxf(x, dpp<HMIR>(x)); x = fmaxf(x, dpp<MIR>(x)); return x;
}
__device__ __forceinline__ float x16sum(float x) {
  auto s = __builtin_amdgcn_permlane16_swap(__float_as_uint(x), __float_as_uint(x), false, false);
  return __uint_as_float(s[0]) + __uint_as_float(s[1]);
}
__device__ __forceinline__ float x32sum(float x) {
  auto s = __builtin_amdgcn_permlane32_swap(__float_as_uint(x), __float_as_uint(x), false, false);
  return __uint_as_float(s[0]) + __uint_as_float(s[1]);
}
__device__ __forceinline__ float x16max(float x) {
  auto s = __builtin_amdgcn_permlane16_swap(__float_as_uint(x), __float_as_uint(x), false, false);
  return fmaxf(__uint_as_float(s[0]), __uint_as_float(s[1]));
}
__device__ __forceinline__ float x32max(float x) {
  auto s = __builtin_amdgcn_permlane32_swap(__float_as_uint(x), __float_as_uint(x), false, false);
  return fmaxf(__uint_as_float(s[0]), __uint_as_float(s[1]));
}
__device__ __forceinline__ float sum32(float x) { return x16sum(sum16(x)); }
__device__ __forceinline__ float sum64(float x) { return x32sum(x16sum(sum16(x))); }
__device__ __forceinline__ float max64(float x) { return x32max(x16max(max16(x))); }
#define SB() __builtin_amdgcn_sched_barrier(0)
template <int I> struct IC { static constexpr int value = I; };
template <int B, int E, typename Fn> __device__ __forceinline__ void static_for(Fn&& f) {
  if constexpr (B < E) { f(IC<B>{}); static_for<B + 1, E>(f); }
}
__device__ __forceinline__ void lds_wait() { asm volatile("s_waitcnt lgkmcnt(0)" ::: "memory"); }
__device__ __forceinline__ void wg_barrier() { lds_wait(); __builtin_amdgcn_s_barrier(); asm volatile("" ::: "memory"); }
__device__ __forceinline__ void put(gu64* g, u32 tag, u32 v) { __hip_atomic_store(g, ((u64)tag << 32) | v, RLX_AGENT); }
__device__ __forceinline__ u64 get(gu64* g) { return __hip_atomic_load(g, RLX_AGENT); }

__device__ __forceinline__ float treduce32(float (&pv)[32], int lane) {
#pragma unroll
  for (int j = 0; j < 16; ++j) {
    auto t = __builtin_amdgcn_permlane32_swap(__float_as_uint(pv[j]), __float_as_uint(pv[j + 16]), false, false);
    pv[j] = __uint_as_float(t[0]) + __uint_as_float(t[1]);
  }
#pragma unroll
  for (int j = 0; j < 8; ++j) {
    auto t = __builtin_amdgcn_permlane16_swap(__float_as_uint(pv[j]), __float_as_uint(pv[j + 8]), false, false);
    pv[j] = __uint_as_float(t[0]) + __uint_as_float(t[1]);
  }
  float mine = 0.f;
#pragma unroll
  for (int j = 0; j < 8; ++j) { const float t = sum16(pv[j]); mine = ((lane & 15) == j) ? t : mine; }
  return mine;
}

#define ST(k)

constexpr int L_Q = 0;
constexpr int L_TAG = L_Q + 128;
constexpr int L_FLG = L_TAG + 4;
constexpr int L_XN = L_FLG + 8;
constexpr int L_SC = L_XN + 2 * D;
constexpr int L_P = L_SC + 1024;
constexpr int L_O = L_P + 8 * 512;
constexpr int L_YP = L_O + 8 * 64;
constexpr int L_YF = L_YP + NW * D;
constexpr int L_RED = L_YF + D;
constexpr int L_WO = L_RED + 8;
constexpr int L_END = L_WO + 64 * 224;
constexpr float C2 = 0.0625f * 1.4426950408889634f;
#define LAS __attribute__((address_space(3)))
__device__ __forceinline__ void lds_spin(float* lds, int idx, u32 want) {
  volatile LAS u32* p = (volatile LAS u32*)(lds + idx);
#pragma unroll 1
  for (unsigned spin = 0; spin < (SPIN_CAP << 4); ++spin) {
    if ((u32)__builtin_amdgcn_readfirstlane(*p) >= want) break;
    __builtin_amdgcn_s_sleep(2);
  }
  asm volatile("" ::: "memory");
}
__device__ __forceinline__ u32x4 layernorm8(const u32x4 xr, int lane) {
  float v[8];
#pragma unroll
  for (int j = 0; j < 4; ++j) { v[2 * j] = bflo(xr[j]); v[2 * j + 1] = bfhi(xr[j]); }
  float sm = ((v[0] + v[1]) + (v[2] + v[3])) + ((v[4] + v[5]) + (v[6] + v[7]));
  sm = lane < 56 ? sm : 0.f;
  const float mean = sum64(sm) * (1.0f / D);
  float sq = 0.f;
#pragma unroll
  for (int j = 0; j < 8; ++j) { const float d = v[j] - mean; sq += d * d; }
  sq = lane < 56 ? sq : 0.f;
  const float var = rbf(sum64(sq) * (1.0f / D));
  const float den = rbf(sqrtf(rbf(var + 1e-5f)));
  const float mb = rbf(mean);
  u32x4 o;
#pragma unroll
  for (int j = 0; j < 4; ++j) {
    const u32 d = cvtpk(v[2 * j] - mb, v[2 * j + 1] - mb);
    o[j] = cvtpk(bflo(d) / den, bfhi(d) / den);
  }
  return o;
}
template <int NP, int OFF, int N>
__device__ __forceinline__ void gemv_pairs(const u32x4 (&t)[2 * NP], const u32 (&xa)[N], const u32 (&xb)[N], float (&pa)[8], float (&pb)[8]) {
#pragma unroll
  for (int i = 0; i < NP; ++i) {
    const u32 x0 = xa[OFF + i], x1 = xb[OFF + i];
#pragma unroll
    for (int j = 0; j < 4; ++j) {
      const u32 lo = __builtin_amdgcn_perm(t[2 * i + 1][j], t[2 * i][j], 0x05040100u), hi = __builtin_amdgcn_perm(t[2 * i + 1][j], t[2 * i][j], 0x07060302u);
      pa[2 * j] = dot2(lo, x0, pa[2 * j]); pa[2 * j + 1] = dot2(hi, x0, pa[2 * j + 1]);
      pb[2 * j] = dot2(lo, x1, pb[2 * j]); pb[2 * j + 1] = dot2(hi, x1, pb[2 * j + 1]);
    }
  }
}
__device__ __forceinline__ void reduce_rg(float (&pa)[8], float (&pb)[8], int lane, float& r0, float& r1) {
#pragma unroll
  for (int j = 0; j < 8; ++j) {
    auto t = __builtin_amdgcn_permlane32_swap(__float_as_uint(pa[j]), __float_as_uint(pb[j]), false, false);
    pa[j] = __uint_as_float(t[0]) + __uint_as_float(t[1]);
  }
#pragma unroll
  for (int j = 0; j < 4; ++j) {
    auto t = __builtin_amdgcn_permlane16_swap(__float_as_uint(pa[j]), __float_as_uint(pa[j + 4]), false, false);
    pa[j] = __uint_as_float(t[0]) + __uint_as_float(t[1]);
  }
  float a0 = pa[0] + dpp<ROR8>(pa[0]), a1 = pa[1] + dpp<ROR8>(pa[1]), a2 = pa[2] + dpp<ROR8>(pa[2]), a3 = pa[3] + dpp<ROR8>(pa[3]);
  asm volatile("" : "+v"(a0), "+v"(a1), "+v"(a2), "+v"(a3));
  const bool bit3 = (lane >> 3) & 1;
  r0 = bit3 ? a2 : a0; r1 = bit3 ? a3 : a1;
}

__global__ void __launch_bounds__(NT) main_k(const u16* __restrict__ x, const u32x4* __restrict__ Kc, const u32x4* __restrict__ Vc,
                                            const u16* __restrict__ wq, const u16* __restrict__ wo, const u16* __restrict__ wg,
                                            const u16* __restrict__ wu, const u16* __restrict__ wd, u64* ws, u16* __restrict__ out) {
  __shared__ __attribute__((aligned(16))) float lds[L_END];
  gu64* const G = (gu64*)ws;
  const int lane = threadIdx.x & 63, half = lane >> 5, l32 = lane & 31;
  const int wv = __builtin_amdgcn_readfirstlane(threadIdx.x >> 6);
  const int bid = blockIdx.x;
  int b, h, s;
  if (MAPX == 0) { b = bid >> 7; h = (bid >> 3) & 15; s = bid & 7; }
  else { const int xcd = bid & 7, idx = bid >> 3; h = xcd * 2 + (idx >> 4); b = (idx >> 3) & 1; s = idx & 7; }
  const int g = b * H + h, slot = g * 8 + s;
  const int l56 = lane < 56 ? lane : 55;
  const int jd = s & 3, jk = s >> 2, rg = lane >> 3;

  if (wv == 0) {
    ST(0);
    __builtin_amdgcn_s_setprio(3);
    const int j16 = b * 8 + s, qc = j16 & 3, qk = j16 >> 2;
    const u32 epoch = __hip_atomic_load((gu32*)(G + G_E), RLX_AGENT);
    const u32x4 xr0 = *reinterpret_cast<const u32x4*>(x + l56 * 8);
    const u32x4 xr1 = *reinterpret_cast<const u32x4*>(x + D + l56 * 8);
    u32x4 qw[14];
    const u32x4* p = reinterpret_cast<const u32x4*>(wq + (size_t)(qk * 112 + rg * 14) * (H * DH) + h * DH + qc * 64) + (lane & 7);
#pragma unroll
    for (int i = 0; i < 14; ++i) qw[i] = __builtin_nontemporal_load(p + i * (H * DH / 8));
    SB();
    if (lane < 8) reinterpret_cast<u32*>(lds)[L_FLG + lane] = 0u;
    {
      const u32x4 n0 = layernorm8(xr0, lane), n1 = layernorm8(xr1, lane);
      if (lane < 56) { *reinterpret_cast<u32x4*>(lds + L_XN + lane * 4) = n0; *reinterpret_cast<u32x4*>(lds + L_XN + 224 + lane * 4) = n1; }
    }
    lds_wait();
    const u32 tag = __builtin_amdgcn_readfirstlane(epoch) + 0x9E3779B9u;
    {
      float pa[8], pb[8], r0, r1;
#pragma unroll
      for (int j = 0; j < 8; ++j) { pa[j] = 0.f; pb[j] = 0.f; }
      const u32* xp = reinterpret_cast<const u32*>(lds + L_XN) + qk * 56 + rg * 7;
      u32 xa[7], xb[7];
#pragma unroll
      for (int i = 0; i < 7; ++i) { xa[i] = xp[i]; xb[i] = xp[224 + i]; }
      gemv_pairs<7, 0, 7>(qw, xa, xb, pa, pb);
      reduce_rg(pa, pb, lane, r0, r1);
      gu64* qo = G + G_Q + ((h * 2 + half) * 4 + qk) * 256 + qc * 64 + l32;
      put(qo, tag, __float_as_uint(r0));
      put(qo + 32, tag, __float_as_uint(r1));
    }
    __builtin_amdgcn_s_sleep(QDELAY);
    ST(1);
    {
      gu64* qg = G + G_Q + (h * 2 + b) * 1024 + half * 128 + l32;
      float qs[4];
#pragma unroll 1
      for (unsigned spin = 0; spin < SPIN_CAP; ++spin) {
        u64 v[16];
#pragma unroll
        for (int k = 0; k < 4; ++k)
#pragma unroll
          for (int j = 0; j < 4; ++j) v[k * 4 + j] = get(qg + k * 256 + j * 32);
        bool ok = true;
#pragma unroll
        for (int k = 0; k < 16; ++k) ok &= (u32)(v[k] >> 32) == tag;
#pragma unroll
        for (int j = 0; j < 4; ++j) qs[j] = (__uint_as_float((u32)v[j]) + __uint_as_float((u32)v[4 + j])) + (__uint_as_float((u32)v[8 + j]) + __uint_as_float((u32)v[12 + j]));
        if (__all(ok)) break;
      }
      const int pr = (l32 & 7) * 4 + ((l32 >> 4) & 1) * 2 + ((l32 >> 3) & 1);
      reinterpret_cast<u32*>(lds + L_Q)[(half * 2) * 32 + pr] = cvtpk(qs[0], qs[1]);
      reinterpret_cast<u32*>(lds + L_Q)[(half * 2 + 1) * 32 + pr] = cvtpk(qs[2], qs[3]);
      if (lane == 0) reinterpret_cast<u32*>(lds)[L_TAG] = tag;
    }
    ST(2);
    wg_barrier();
    float mA = 0.f, M1 = 0.f;
#pragma unroll 1
    for (int st = 0; st < 2; ++st) {
      lds_spin(lds, L_FLG + st, 8u);
      {
        u32 sp[4]; float mx = -INFINITY;
#pragma unroll
        for (int k = 0; k < 4; ++k) {
          sp[k] = cvtpk(lds[L_SC + st * 512 + (2 * k) * 64 + lane], lds[L_SC + st * 512 + (2 * k + 1) * 64 + lane]);
          put(G + G_S + ((st * 256 + slot) * 4 + k) * 64 + lane, tag, sp[k]);
          mx = fmaxf(mx, fmaxf(bflo(sp[k]), bfhi(sp[k])));
        }
        mx = max64(mx) * C2;
        float ls = 0.f;
#pragma unroll
        for (int k = 0; k < 4; ++k) ls += __builtin_amdgcn_exp2f(bflo(sp[k]) * C2 - mx) + __builtin_amdgcn_exp2f(bfhi(sp[k]) * C2 - mx);
        ls = sum64(ls);
        if (lane < 2) put(G + G_ST + (st * 32 + g) * 16 + s * 2 + lane, tag, __float_as_uint(lane ? ls : mx));
      }
      lds_spin(lds, L_FLG + 5, 3u + 2u * st);
      gu64* tg = G + G_ST + (st * 32 + g) * 16 + (lane & 15);
      gu64* sg = G + G_S + (st * 256 + g * 8 + jk * 4) * 256 + lane;
      float sv = 0.f;
      u32 sc[16];
#pragma unroll 1
      for (unsigned spin = 0; spin < SPIN_CAP; ++spin) {
        const u64 a = get(tg);
        sv = __uint_as_float((u32)a);
        bool ok = (u32)(a >> 32) == tag;
#pragma unroll
        for (int k = 0; k < 16; ++k) { const u64 v = get(sg + k * 64); sc[k] = (u32)v; ok &= (u32)(v >> 32) == tag; }
        if (__all(ok)) break;
        __builtin_amdgcn_s_sleep(POLLSLEEP);
      }
      const bool isl = lane & 1;
      float M = max16(isl ? -INFINITY : sv);
      if (st == 0) { mA = sv; M1 = M; }
      else {
        M = fmaxf(M, M1);
        const float ea = __builtin_amdgcn_exp2f(dpp<XOR1>(mA) - M) * mA, eb = __builtin_amdgcn_exp2f(dpp<XOR1>(sv) - M) * sv;
        const float Li = 1.0f / sum16(isl ? ea + eb : 0.f);
        if (lane == 0) { lds[L_TAG + 1] = __builtin_amdgcn_exp2f(M1 - M); lds[L_TAG + 2] = Li; }
      }
      float* pw = lds + L_P + st * 256 + (half * 4 + (l32 & 3)) * 8 + (l32 >> 2);
#pragma unroll
      for (int k = 0; k < 16; ++k) {
        pw[(2 * (k & 3)) * 512 + (k >> 2) * 64] = __builtin_amdgcn_exp2f(bflo(sc[k]) * C2 - M);
        pw[(2 * (k & 3) + 1) * 512 + (k >> 2) * 64] = __builtin_amdgcn_exp2f(bfhi(sc[k]) * C2 - M);
      }
      lds_wait();
      if (lane == 0) *(volatile LAS u32*)(lds + L_FLG + 2 + st) = 1u;
    }
    return;
  }
  if (wv == 1) {
    wg_barrier();
    const u32 tag = __builtin_amdgcn_readfirstlane(reinterpret_cast<const u32*>(lds)[L_TAG]);
    float y[8];
#pragma unroll
    for (int j = 0; j < 8; ++j) y[j] = 0.f;
    if (g < 28) {
      const int rr = rg < 7 ? rg : 6;
      const u32* xp = reinterpret_cast<const u32*>(lds + L_XN) + s * 28 + rr * 4;
      u32 xa[4], xb[4];
#pragma unroll
      for (int i = 0; i < 4; ++i) { xa[i] = rg < 7 ? xp[i] : 0u; xb[i] = rg < 7 ? xp[224 + i] : 0u; }
#pragma unroll 1
      for (int mat = 0; mat < 2; ++mat) {
        const u32x4* p = reinterpret_cast<const u32x4*>((mat ? wu : wg) + (size_t)(s * 56 + rr * 8) * F + g * 64) + (lane & 7);
        float pa[8], pb[8], r0, r1;
#pragma unroll
        for (int j = 0; j < 8; ++j) { pa[j] = 0.f; pb[j] = 0.f; }
        static_for<0, 2>([&](auto hc) {
          constexpr int hh = decltype(hc)::value;
          u32x4 t[4];
#pragma unroll
          for (int i = 0; i < 4; ++i) t[i] = __builtin_nontemporal_load(p + (hh * 4 + i) * (F / 8));
          gemv_pairs<2, 2 * hh, 4>(t, xa, xb, pa, pb);
          SB();
        });
        reduce_rg(pa, pb, lane, r0, r1);
        gu64* fo = G + G_F + (((g * 2 + half) * 8 + s) * 2 + mat) * 64 + l32;
        put(fo, tag, __float_as_uint(r0));
        put(fo + 32, tag, __float_as_uint(r1));
      }
    }
    const int ib = h * 8 + s;
    if (ib < 112) {
      const int u = lane & 15, col = (ib & 3) * 16 + u;
      const int pos = (col & 1) * 32 + (col >> 3) + ((col >> 2) & 1) * 16 + ((col >> 1) & 1) * 8;
      gu64* fg = G + G_F + ((((ib >> 2) * 2 + b) * 8 + half * 4) * 2 + ((lane >> 4) & 1)) * 64 + pos;
      float sum = 0.f;
      lds_spin(lds, L_FLG + 5, 2u);
#pragma unroll 1
      for (unsigned spin = 0; spin < SPIN_CAP; ++spin) {
        bool ok = true; sum = 0.f;
#pragma unroll
        for (int k = 0; k < 4; ++k) { const u64 v = get(fg + k * 128); sum += __uint_as_float((u32)v); ok &= (u32)(v >> 32) == tag; }
        if (__all(ok)) break;
        __builtin_amdgcn_s_sleep(64);
      }
      sum = rbf(x32sum(sum));
      auto sw = __builtin_amdgcn_permlane16_swap(__float_as_uint(sum), __float_as_uint(sum), false, false);
      const float gv = __uint_as_float(sw[0]), uv = __uint_as_float(sw[1]);
      const float hv = rbf(rbf(gv / (1.f + __expf(-gv))) * uv);
      const u32x4* pd = reinterpret_cast<const u32x4*>(wd + (size_t)(ib * 16) * D) + l56;
#pragma unroll 1
      for (int u4 = 0; u4 < 4; ++u4) {
        u32x4 dr[4];
#pragma unroll
        for (int k = 0; k < 4; ++k) dr[k] = __builtin_nontemporal_load(pd + (u4 * 4 + k) * (D / 8));
#pragma unroll
        for (int k = 0; k < 4; ++k) {
          const float hu = __uint_as_float(__builtin_amdgcn_readlane(__float_as_uint(hv), u4 * 4 + k));
#pragma unroll
          for (int j = 0; j < 4; ++j) { y[2 * j] = fmaf(hu, bflo(dr[k][j]), y[2 * j]); y[2 * j + 1] = fmaf(hu, bfhi(dr[k][j]), y[2 * j + 1]); }
        }
      }
    }
    if (lane < 56) {
      *reinterpret_cast<f32x4*>(lds + L_YF + lane * 8) = f32x4{y[0], y[1], y[2], y[3]};
      *reinterpret_cast<f32x4*>(lds + L_YF + lane * 8 + 4) = f32x4{y[4], y[5], y[6], y[7]};
    }
    {
      const u32x4* wop = reinterpret_cast<const u32x4*>(wo + (size_t)(h * DH + jd * 64) * D) + lane;
      u32x4* dst = reinterpret_cast<u32x4*>(lds + L_WO) + lane;
#pragma unroll 1
      for (int i = 0; i < 56; i += WOB) {
        u32x4 t[WOB];
#pragma unroll
        for (int k = 0; k < WOB; ++k) t[k] = WONT ? __builtin_nontemporal_load(wop + (i + k) * 64) : wop[(i + k) * 64];
#pragma unroll
        for (int k = 0; k < WOB; ++k) dst[(i + k) * 64] = t[k];
      }
    }
    lds_wait();
    if (lane == 0) *(volatile LAS u32*)(lds + L_FLG + 4) = 1u;
    return;
  }

  const int w = wv - 2, tid = threadIdx.x - 128;
  const GAS char* const kbase = (const GAS char*)Kc + (size_t)slot * (1024 * 512) + w * 1024;
  const GAS char* const vbase = (const GAS char*)Vc + (size_t)g * (SK * 512) + (size_t)jk * (4096 * 512) + jd * 128 + w * 1024;
  const u32 koff = lane * 16, voff = ((rg >> 1) * 16 + (rg & 1)) * 512 + (lane & 7) * 16;
  u32x4 buf[RING];
  const GAS char* lp = kbase; u32 loff = koff; int S0 = 8192, SX = 0;
  auto issue = [&](auto jc) {
    constexpr int j = decltype(jc)::value;
    asm volatile("s_waitcnt vmcnt(%0)" :: "i"(INFL - 1) : "memory");
    asm volatile("" : "+s"(lp));
    buf[j] = __builtin_nontemporal_load((const GAS u32x4*)(lp + loff));
    lp += S0; if constexpr (j == 7) lp += SX;
  };
  static_for<0, RING>(issue);
  SB();
  ST(3);
  wg_barrier();
  const u32x4 qp = *reinterpret_cast<const u32x4*>(lds + L_Q + l32 * 4);
  const u32 tag = __builtin_amdgcn_readfirstlane(reinterpret_cast<const u32*>(lds)[L_TAG]);
  ST(4);

  float sr = 0.f;
#pragma unroll 1
  for (int it = 0; it < 4; ++it) {
    if (it == 3) { lp = vbase; loff = voff; S0 = 32768; SX = 524288 - 8 * 32768; }
    const int lj = l32 - (it & 1) * 16;
    static_for<0, RING>([&](auto jc) {
      constexpr int j = decltype(jc)::value;
      const u32x4 kv = buf[j];
      float sacc = dot2(kv[0], qp[0], 0.f);
      sacc = dot2(kv[1], qp[1], sacc); sacc = dot2(kv[2], qp[2], sacc); sacc = dot2(kv[3], qp[3], sacc);
      asm volatile("" : "+v"(sacc));
      issue(jc);
      sacc = sum32(sacc);
      sr = (lj == j) ? sacc : sr;
      SB();
    });
    if (it & 1) {
      lds[L_SC + (it >> 1) * 512 + tid] = sr;
      lds_wait();
      if (lane == 0) __hip_atomic_fetch_add((LAS u32*)(lds + L_FLG + (it >> 1)), 1u, __ATOMIC_RELAXED, __HIP_MEMORY_SCOPE_WORKGROUP);
    }
    if (tid == 0) *(volatile LAS u32*)(lds + L_FLG + 5) = it + 1;
    if (it == 1) ST(6);
    SB();
  }
  ST(5);
  f32x2 ac[4];
#pragma unroll
  for (int j = 0; j < 4; ++j) ac[j] = f32x2{0.f, 0.f};
  const float* pr_base = lds + L_P + w * 512 + ((rg & 1) * 4 + (rg >> 1)) * 8;
  auto vbody = [&](auto refill, const float* pp) {
    f32x4 pr;
    static_for<0, RING>([&](auto jc) {
      constexpr int j = decltype(jc)::value;
      if constexpr ((j & 3) == 0) pr = *reinterpret_cast<const f32x4*>(pp + (j >> 3) * 64 + (j & 7));
      const u32x4 vv = buf[j];
      const float p = pr[j & 3];
#pragma unroll
      for (int k = 0; k < 4; ++k) ac[k] = __builtin_elementwise_fma(f32x2{p, p}, f32x2{bflo(vv[k]), bfhi(vv[k])}, ac[k]);
      asm volatile("" : "+v"(ac[0]), "+v"(ac[1]), "+v"(ac[2]), "+v"(ac[3]));
      if constexpr (decltype(refill)::value) issue(jc);
      SB();
    });
  };
#pragma unroll 1
  for (int it = 0; it < 3; ++it) {
    if ((it & 1) == 0) {
      lds_spin(lds, L_FLG + 2 + (it >> 1), 1u);
      if (it == 2) {
        const float sc = lds[L_TAG + 1];
#pragma unroll
        for (int k = 0; k < 4; ++k) ac[k] *= sc;
      }
    }
    if (it == 2) ST(7);
    lp = vbase + ((it + 1) & 1) * 1048576 + ((it + 1) >> 1) * 262144;
    vbody(IC<1>{}, pr_base + it * 128);
    if (tid == 0) *(volatile LAS u32*)(lds + L_FLG + 5) = it + 5;
  }
  ST(8);
  lds_spin(lds, L_FLG + 4, 1u);
  u32x4 wreg[8];
#pragma unroll
  for (int k = 0; k < 4; ++k) {
    const u32x4 a = *(reinterpret_cast<const u32x4*>(lds + L_WO) + (8 * w + 2 * k) * 56 + l56), c = *(reinterpret_cast<const u32x4*>(lds + L_WO) + (8 * w + 2 * k + 1) * 56 + l56);
    u32x4 lo, hi;
#pragma unroll
    for (int j = 0; j < 4; ++j) { lo[j] = __builtin_amdgcn_perm(c[j], a[j], 0x05040100u); hi[j] = __builtin_amdgcn_perm(c[j], a[j], 0x07060302u); }
    wreg[2 * k] = lo; wreg[2 * k + 1] = hi;
  }
  const float Linv = lds[L_TAG + 2];
  SB();
  vbody(IC<0>{}, pr_base + 3 * 128);
  ST(9);

  {
    float a[8];
#pragma unroll
    for (int j = 0; j < 4; ++j) { a[2 * j] = ac[j][0]; a[2 * j + 1] = ac[j][1]; }
#pragma unroll
    for (int j = 0; j < 8; ++j) { a[j] += dpp<ROR8>(a[j]); a[j] = x32sum(x16sum(a[j])); }
    if (lane < 8) {
      *reinterpret_cast<f32x4*>(lds + L_O + w * 64 + lane * 8) = f32x4{a[0], a[1], a[2], a[3]};
      *reinterpret_cast<f32x4*>(lds + L_O + w * 64 + lane * 8 + 4) = f32x4{a[4], a[5], a[6], a[7]};
    }
    wg_barrier();
    float o = lds[L_O + (lane >> 3) * 64 + 8 * w + (lane & 7)];
    o += dpp<ROR8>(o); o = x32sum(x16sum(o)) * Linv;
    const u32 opk = cvtpk(o, dpp<XOR1>(o));
    float y[8];
#pragma unroll
    for (int j = 0; j < 8; ++j) y[j] = 0.f;
#pragma unroll
    for (int k = 0; k < 4; ++k) {
      const u32 ok = __builtin_amdgcn_readlane(opk, 2 * k);
#pragma unroll
      for (int j = 0; j < 4; ++j) { y[2 * j] = dot2(wreg[2 * k][j], ok, y[2 * j]); y[2 * j + 1] = dot2(wreg[2 * k + 1][j], ok, y[2 * j + 1]); }
    }
    if (lane < 56) {
      *reinterpret_cast<f32x4*>(lds + L_YP + w * D + lane * 8) = f32x4{y[0], y[1], y[2], y[3]};
      *reinterpret_cast<f32x4*>(lds + L_YP + w * D + lane * 8 + 4) = f32x4{y[4], y[5], y[6], y[7]};
    }
    wg_barrier();
    if (tid < D) {
      float t = lds[L_YF + tid];
#pragma unroll
      for (int ww = 0; ww < NW; ++ww) t += lds[L_YP + ww * D + tid];
      put(G + G_Y + ((b * 112 + (tid >> 2)) * 128 + (slot & 127)) * 4 + (tid & 3), tag, __float_as_uint(t));
    }
  }
  ST(10);

  if (bid < 224) {
    const int bc = bid / 112, cg = bid % 112;
    if (w < 2) {
      gu64* src = G + G_Y + (bid * 128 + tid) * 4;
      float v[4] = {0.f, 0.f, 0.f, 0.f};
      bool ok = false;
#pragma unroll 1
      for (unsigned spin = 0; spin < SPIN_CAP; ++spin) {
        if (!ok) {
          ok = true;
#pragma unroll
          for (int k = 0; k < 4; ++k) { const u64 t = get(src + k); v[k] = __uint_as_float((u32)t); ok &= (u32)(t >> 32) == tag; }
        }
        if (__all(ok)) break;
        __builtin_amdgcn_s_sleep(2);
      }
#pragma unroll
      for (int k = 0; k < 4; ++k) v[k] = sum64(v[k]);
      if (lane == 0) *reinterpret_cast<f32x4*>(lds + L_RED + w * 4) = f32x4{v[0], v[1], v[2], v[3]};
    }
    wg_barrier();
    if (tid < 4) {
      const float xv = __uint_as_float((u32)x[bc * D + cg * 4 + tid] << 16);
      const float r = xv + (lds[L_RED + tid] + lds[L_RED + 4 + tid]);
      out[bc * D + cg * 4 + tid] = (u16)(cvtpk(r, r) & 0xffffu);
    }
  } else if (bid == NWG - 1) {
    if (w < 4) {
      bool ok = false;
#pragma unroll 1
      for (unsigned spin = 0; spin < SPIN_CAP; ++spin) {
        if (!ok) ok = (u32)(get(G + G_Y + ((tid >> 7) * 112 * 128 + (tid & 127)) * 4) >> 32) == tag;
        if (__all(ok)) break;
        __builtin_amdgcn_s_sleep(2);
      }
    }
    wg_barrier();
    if (tid == 0) __hip_atomic_store(G + G_E, (u64)tag, RLX_AGENT);
  }
  ST(11);
}

extern "C" void kernel_launch(void* const* d_in, const int* in_sizes, int n_in,
                              void* d_out, int out_size, void* d_ws, size_t ws_size,
                              hipStream_t stream) {
  main_k<<<NWG, NT, 0, stream>>>((const u16*)d_in[0], (const u32x4*)d_in[1], (const u32x4*)d_in[2], (const u16*)d_in[3],
                                 (const u16*)d_in[4], (const u16*)d_in[6], (const u16*)d_in[7], (const u16*)d_in[8],
                                 (u64*)d_ws, (u16*)d_out);
}
```

```cpp
#include <hip/hip_runtime.h>
#include <hip/hip_bf16.h>
#include <stdint.h>
#include <stdio.h>

#define KMODE 0
#define VMODE 0
#define MAPX 1
#define INFL 3
#define WONT 1
#define WOB 4
#define QDELAY 16
#define POLLSLEEP 8

typedef unsigned int u32;
typedef unsigned short u16;
typedef unsigned long long u64;
typedef __attribute__((ext_vector_type(4))) unsigned u32x4;
typedef __attribute__((ext_vector_type(4))) float f32x4;
typedef __attribute__((ext_vector_type(2))) float f32x2;
typedef __attribute__((ext_vector_type(2))) __bf16 bf16x2;
#define GAS __attribute__((address_space(1)))
typedef GAS u32 gu32;
typedef GAS u64 gu64;
#define RLX_AGENT __ATOMIC_RELAXED, __HIP_MEMORY_SCOPE_AGENT

constexpr int D = 448, H = 16, SK = 8192, DH = 256, F = 1792;
constexpr int NWG = 256, NT = 640, NW = 8, RING = 16;
constexpr int NLD = 64;
constexpr unsigned SPIN_CAP = 1u << 18;

constexpr int G_E = 0;
constexpr int G_Q = 32;
constexpr int G_ST = G_Q + 16 * 2 * 4 * 256;
constexpr int G_S = G_ST + 2 * 32 * 16;
constexpr int G_F = G_S + 2 * 256 * 256;
constexpr int G_Y = G_F + 28 * 2 * 8 * 128;

__device__ __forceinline__ float bflo(u32 v) { return __uint_as_float(v << 16); }
__device__ __forceinline__ float bfhi(u32 v) { return __uint_as_float(v & 0xffff0000u); }
__device__ __forceinline__ u32 cvtpk(float lo, float hi) {
  u32 r; asm("v_cvt_pk_bf16_f32 %0, %1, %2" : "=v"(r) : "v"(lo), "v"(hi)); return r;
}
__device__ __forceinline__ float rbf(float f) { return bflo(cvtpk(f, f)); }
__device__ __forceinline__ float dot2(u32 a, u32 b, float c) {
  return __builtin_amdgcn_fdot2_f32_bf16(__builtin_bit_cast(bf16x2, a), __builtin_bit_cast(bf16x2, b), c, false);
}
template <int CTRL> __device__ __forceinline__ float dpp(float x) {
  return __builtin_bit_cast(float, __builtin_amdgcn_mov_dpp(__builtin_bit_cast(int, x), CTRL, 0xf, 0xf, true));
}
constexpr int XOR1 = 0xB1, XOR2 = 0x4E, HMIR = 0x141, MIR = 0x140, ROR8 = 0x128;
__device__ __forceinline__ float sum16(float x) {
  x += dpp<XOR1>(x); x += dpp<XOR2>(x); x += dpp<HMIR>(x); x += dpp<MIR>(x); return x;
}
__device__ __forceinline__ float max16(float x) {
  x = fmaxf(x, dpp<XOR1>(x)); x = fmaxf(x, dpp<XOR2>(x)); x = fmaxf(x, dpp<HMIR>(x)); x = fmaxf(x, dpp<MIR>(x)); return x;
}
__device__ __forceinline__ float x16sum(float x) {
  auto s = __builtin_amdgcn_permlane16_swap(__float_as_uint(x), __float_as_uint(x), false, false);
  return __uint_as_float(s[0]) + __uint_as_float(s[1]);
}
__device__ __forceinline__ float x32sum(float x) {
  auto s = __builtin_amdgcn_permlane32_swap(__float_as_uint(x), __float_as_uint(x), false, false);
  return __uint_as_float(s[0]) + __uint_as_float(s[1]);
}
__device__ __forceinline__ float x16max(float x) {
  auto s = __builtin_amdgcn_permlane16_swap(__float_as_uint(x), __float_as_uint(x), false, false);
  return fmaxf(__uint_as_float(s[0]), __uint_as_float(s[1]));
}
__device__ __forceinline__ float x32max(float x) {
  auto s = __builtin_amdgcn_permlane32_swap(__float_as_uint(x), __float_as_uint(x), false, false);
  return fmaxf(__uint_as_float(s[0]), __uint_as_float(s[1]));
}
__device__ __forceinline__ float sum32(float x) { return x16sum(sum16(x)); }
__device__ __forceinline__ float sum64(float x) { return x32sum(x16sum(sum16(x))); }
__device__ __forceinline__ float max64(float x) { return x32max(x16max(max16(x))); }
#define SB() __builtin_amdgcn_sched_barrier(0)
template <int I> struct IC { static constexpr int value = I; };
template <int B, int E, typename Fn> __device__ __forceinline__ void static_for(Fn&& f) {
  if constexpr (B < E) { f(IC<B>{}); static_for<B + 1, E>(f); }
}
__device__ __forceinline__ void lds_wait() { asm volatile("s_waitcnt lgkmcnt(0)" ::: "memory"); }
__device__ __forceinline__ void wg_barrier() { lds_wait(); __builtin_amdgcn_s_barrier(); asm volatile("" ::: "memory"); }
__device__ __forceinline__ void put(gu64* g, u32 tag, u32 v) { __hip_atomic_store(g, ((u64)tag << 32) | v, RLX_AGENT); }
__device__ __forceinline__ u64 get(gu64* g) { return __hip_atomic_load(g, RLX_AGENT); }

__device__ __forceinline__ float treduce32(float (&pv)[32], int lane) {
#pragma unroll
  for (int j = 0; j < 16; ++j) {
    auto t = __builtin_amdgcn_permlane32_swap(__float_as_uint(pv[j]), __float_as_uint(pv[j + 16]), false, false);
    pv[j] = __uint_as_float(t[0]) + __uint_as_float(t[1]);
  }
#pragma unroll
  for (int j = 0; j < 8; ++j) {
    auto t = __builtin_amdgcn_permlane16_swap(__float_as_uint(pv[j]), __float_as_uint(pv[j + 8]), false, false);
    pv[j] = __uint_as_float(t[0]) + __uint_as_float(t[1]);
  }
  float mine = 0.f;
#pragma unroll
  for (int j = 0; j < 8; ++j) { const float t = sum16(pv[j]); mine = ((lane & 15) == j) ? t : mine; }
  return mine;
}

#define ST(k)

constexpr int L_Q = 0;
constexpr int L_TAG = L_Q + 128;
constexpr int L_FLG = L_TAG + 4;
constexpr int L_XN = L_FLG + 8;
constexpr int L_SC = L_XN + 2 * D;
constexpr int L_P = L_SC + 1024;
constexpr int L_O = L_P + 8 * 512;
constexpr int L_YP = L_O + 8 * 64;
constexpr int L_YF = L_YP + NW * D;
constexpr int L_RED = L_YF + D;
constexpr int L_WO = L_RED + 8;
constexpr int L_END = L_WO + 64 * 224;
constexpr float C2 = 0.0625f * 1.4426950408889634f;
#define LAS __attribute__((address_space(3)))
__device__ __forceinline__ void lds_spin(float* lds, int idx, u32 want) {
  volatile LAS u32* p = (volatile LAS u32*)(lds + idx);
#pragma unroll 1
  for (unsigned spin = 0; spin < (SPIN_CAP << 4); ++spin) {
    if ((u32)__builtin_amdgcn_readfirstlane(*p) >= want) break;
    __builtin_amdgcn_s_sleep(2);
  }
  asm volatile("" ::: "memory");
}
__device__ __forceinline__ u32x4 layernorm8(const u32x4 xr, int lane) {
  float v[8];
#pragma unroll
  for (int j = 0; j < 4; ++j) { v[2 * j] = bflo(xr[j]); v[2 * j + 1] = bfhi(xr[j]); }
  float sm = ((v[0] + v[1]) + (v[2] + v[3])) + ((v[4] + v[5]) + (v[6] + v[7]));
  sm = lane < 56 ? sm : 0.f;
  const float mean = sum64(sm) * (1.0f / D);
  float sq = 0.f;
#pragma unroll
  for (int j = 0; j < 8; ++j) { const float d = v[j] - mean; sq += d * d; }
  sq = lane < 56 ? sq : 0.f;
  const float var = rbf(sum64(sq) * (1.0f / D));
  const float den = rbf(sqrtf(rbf(var + 1e-5f)));
  const float mb = rbf(mean);
  u32x4 o;
#pragma unroll
  for (int j = 0; j < 4; ++j) {
    const u32 d = cvtpk(v[2 * j] - mb, v[2 * j + 1] - mb);
    o[j] = cvtpk(bflo(d) / den, bfhi(d) / den);
  }
  return o;
}
template <int NP, int OFF, int N>
__device__ __forceinline__ void gemv_pairs(const u32x4 (&t)[2 * NP], const u32 (&xa)[N], const u32 (&xb)[N], float (&pa)[8], float (&pb)[8]) {
#pragma unroll
  for (int i = 0; i < NP; ++i) {
    const u32 x0 = xa[OFF + i], x1 = xb[OFF + i];
#pragma unroll
    for (int j = 0; j < 4; ++j) {
      const u32 lo = __builtin_amdgcn_perm(t[2 * i + 1][j], t[2 * i][j], 0x05040100u), hi = __builtin_amdgcn_perm(t[2 * i + 1][j], t[2 * i][j], 0x07060302u);
      pa[2 * j] = dot2(lo, x0, pa[2 * j]); pa[2 * j + 1] = dot2(hi, x0, pa[2 * j + 1]);
      pb[2 * j] = dot2(lo, x1, pb[2 * j]); pb[2 * j + 1] = dot2(hi, x1, pb[2 * j + 1]);
    }
  }
}
__device__ __forceinline__ void reduce_rg(float (&pa)[8], float (&pb)[8], int lane, float& r0, float& r1) {
#pragma unroll
  for (int j = 0; j < 8; ++j) {
    auto t = __builtin_amdgcn_permlane32_swap(__float_as_uint(pa[j]), __float_as_uint(pb[j]), false, false);
    pa[j] = __uint_as_float(t[0]) + __uint_as_float(t[1]);
  }
#pragma unroll
  for (int j = 0; j < 4; ++j) {
    auto t = __builtin_amdgcn_permlane16_swap(__float_as_uint(pa[j]), __float_as_uint(pa[j + 4]), false, false);
    pa[j] = __uint_as_float(t[0]) + __uint_as_float(t[1]);
  }
  float a0 = pa[0] + dpp<ROR8>(pa[0]), a1 = pa[1] + dpp<ROR8>(pa[1]), a2 = pa[2] + dpp<ROR8>(pa[2]), a3 = pa[3] + dpp<ROR8>(pa[3]);
  asm volatile("" : "+v"(a0), "+v"(a1), "+v"(a2), "+v"(a3));
  const bool bit3 = (lane >> 3) & 1;
  r0 = bit3 ? a2 : a0; r1 = bit3 ? a3 : a1;
}

__global__ void __launch_bounds__(NT) main_k(const u16* __restrict__ x, const u32x4* __restrict__ Kc, const u32x4* __restrict__ Vc,
                                            const u16* __restrict__ wq, const u16* __restrict__ wo, const u16* __restrict__ wg,
                                            const u16* __restrict__ wu, const u16* __restrict__ wd, u64* ws, u16* __restrict__ out) {
  __shared__ __attribute__((aligned(16))) float lds[L_END];
  gu64* const G = (gu64*)ws;
  const int lane = threadIdx.x & 63, half = lane >> 5, l32 = lane & 31;
  const int wv = __builtin_amdgcn_readfirstlane(threadIdx.x >> 6);
  const int bid = blockIdx.x;
  int b, h, s;
  if (MAPX == 0) { b = bid >> 7; h = (bid >> 3) & 15; s = bid & 7; }
  else { const int xcd = bid & 7, idx = bid >> 3; h = xcd * 2 + (idx >> 4); b = (idx >> 3) & 1; s = idx & 7; }
  const int g = b * H + h, slot = g * 8 + s;
  const int l56 = lane < 56 ? lane : 55;
  const int jd = s & 3, jk = s >> 2, rg = lane >> 3;

  if (wv == 0) {
    ST(0);
    __builtin_amdgcn_s_setprio(3);
    const int j16 = b * 8 + s, qc = j16 & 3, qk = j16 >> 2;
    const u32 epoch = __hip_atomic_load((gu32*)(G + G_E), RLX_AGENT);
    const u32x4 xr0 = *reinterpret_cast<const u32x4*>(x + l56 * 8);
    const u32x4 xr1 = *reinterpret_cast<const u32x4*>(x + D + l56 * 8);
    u32x4 qw[14];
    const u32x4* p = reinterpret_cast<const u32x4*>(wq + (size_t)(qk * 112 + rg * 14) * (H * DH) + h * DH + qc * 64) + (lane & 7);
#pragma unroll
    for (int i = 0; i < 14; ++i) qw[i] = __builtin_nontemporal_load(p + i * (H * DH / 8));
    SB();
    if (lane < 8) reinterpret_cast<u32*>(lds)[L_FLG + lane] = 0u;
    {
      const u32x4 n0 = layernorm8(xr0, lane), n1 = layernorm8(xr1, lane);
      if (lane < 56) { *reinterpret_cast<u32x4*>(lds + L_XN + lane * 4) = n0; *reinterpret_cast<u32x4*>(lds + L_XN + 224 + lane * 4) = n1; }
    }
    lds_wait();
    const u32 tag = __builtin_amdgcn_readfirstlane(epoch) + 0x9E3779B9u;
    {
      float pa[8], pb[8], r0, r1;
#pragma unroll
      for (int j = 0; j < 8; ++j) { pa[j] = 0.f; pb[j] = 0.f; }
      const u32* xp = reinterpret_cast<const u32*>(lds + L_XN) + qk * 56 + rg * 7;
      u32 xa[7], xb[7];
#pragma unroll
      for (int i = 0; i < 7; ++i) { xa[i] = xp[i]; xb[i] = xp[224 + i]; }
      gemv_pairs<7, 0, 7>(qw, xa, xb, pa, pb);
      reduce_rg(pa, pb, lane, r0, r1);
      gu64* qo = G + G_Q + ((h * 2 + half) * 4 + qk) * 256 + qc * 64 + l32;
      put(qo, tag, __float_as_uint(r0));
      put(qo + 32, tag, __float_as_uint(r1));
    }
    __builtin_amdgcn_s_sleep(QDELAY);
    ST(1);
    {
      gu64* qg = G + G_Q + (h * 2 + b) * 1024 + half * 128 + l32;
      float qs[4];
#pragma unroll 1
      for (unsigned spin = 0; spin < SPIN_CAP; ++spin) {
        u64 v[16];
#pragma unroll
        for (int k = 0; k < 4; ++k)
#pragma unroll
          for (int j = 0; j < 4; ++j) v[k * 4 + j] = get(qg + k * 256 + j * 32);
        bool ok = true;
#pragma unroll
        for (int k = 0; k < 16; ++k) ok &= (u32)(v[k] >> 32) == tag;
#pragma unroll
        for (int j = 0; j < 4; ++j) qs[j] = (__uint_as_float((u32)v[j]) + __uint_as_float((u32)v[4 + j])) + (__uint_as_float((u32)v[8 + j]) + __uint_as_float((u32)v[12 + j]));
        if (__all(ok)) break;
      }
      const int pr = (l32 & 7) * 4 + ((l32 >> 4) & 1) * 2 + ((l32 >> 3) & 1);
      reinterpret_cast<u32*>(lds + L_Q)[(half * 2) * 32 + pr] = cvtpk(qs[0], qs[1]);
      reinterpret_cast<u32*>(lds + L_Q)[(half * 2 + 1) * 32 + pr] = cvtpk(qs[2], qs[3]);
      if (lane == 0) reinterpret_cast<u32*>(lds)[L_TAG] = tag;
    }
    ST(2);
    wg_barrier();
    float mA = 0.f, M1 = 0.f;
#pragma unroll 1
    for (int st = 0; st < 2; ++st) {
      lds_spin(lds, L_FLG + st, 8u);
      {
        u32 sp[4]; float mx = -INFINITY;
#pragma unroll
        for (int k = 0; k < 4; ++k) {
          sp[k] = cvtpk(lds[L_SC + st * 512 + (2 * k) * 64 + lane], lds[L_SC + st * 512 + (2 * k + 1) * 64 + lane]);
          put(G + G_S + ((st * 256 + slot) * 4 + k) * 64 + lane, tag, sp[k]);
          mx = fmaxf(mx, fmaxf(bflo(sp[k]), bfhi(sp[k])));
        }
        mx = max64(mx) * C2;
        float ls = 0.f;
#pragma unroll
        for (int k = 0; k < 4; ++k) ls += __builtin_amdgcn_exp2f(bflo(sp[k]) * C2 - mx) + __builtin_amdgcn_exp2f(bfhi(sp[k]) * C2 - mx);
        ls = sum64(ls);
        if (lane < 2) put(G + G_ST + (st * 32 + g) * 16 + s * 2 + lane, tag, __float_as_uint(lane ? ls : mx));
      }
      lds_spin(lds, L_FLG + 5, 3u + 2u * st);
      gu64* tg = G + G_ST + (st * 32 + g) * 16 + (lane & 15);
      gu64* sg = G + G_S + (st * 256 + g * 8 + jk * 4) * 256 + lane;
      float sv = 0.f;
      u32 sc[16];
#pragma unroll 1
      for (unsigned spin = 0; spin < SPIN_CAP; ++spin) {
        const u64 a = get(tg);
        sv = __uint_as_float((u32)a);
        bool ok = (u32)(a >> 32) == tag;
#pragma unroll
        for (int k = 0; k < 16; ++k) { const u64 v = get(sg + k * 64); sc[k] = (u32)v; ok &= (u32)(v >> 32) == tag; }
        if (__all(ok)) break;
        __builtin_amdgcn_s_sleep(POLLSLEEP);
      }
      const bool isl = lane & 1;
      float M = max16(isl ? -INFINITY : sv);
      if (st == 0) { mA = sv; M1 = M; }
      else {
        M = fmaxf(M, M1);
        const float ea = __builtin_amdgcn_exp2f(dpp<XOR1>(mA) - M) * mA, eb = __builtin_amdgcn_exp2f(dpp<XOR1>(sv) - M) * sv;
        const float Li = 1.0f / sum16(isl ? ea + eb : 0.f);
        if (lane == 0) { lds[L_TAG + 1] = __builtin_amdgcn_exp2f(M1 - M); lds[L_TAG + 2] = Li; }
      }
      float* pw = lds + L_P + st * 256 + (half * 4 + (l32 & 3)) * 8 + (l32 >> 2);
#pragma unroll
      for (int k = 0; k < 16; ++k) {
        pw[(2 * (k & 3)) * 512 + (k >> 2) * 64] = __builtin_amdgcn_exp2f(bflo(sc[k]) * C2 - M);
        pw[(2 * (k & 3) + 1) * 512 + (k >> 2) * 64] = __builtin_amdgcn_exp2f(bfhi(sc[k]) * C2 - M);
      }
      lds_wait();
      if (lane == 0) *(volatile LAS u32*)(lds + L_FLG + 2 + st) = 1u;
    }
    return;
  }
  if (wv == 1) {
    wg_barrier();
    const u32 tag = __builtin_amdgcn_readfirstlane(reinterpret_cast<const u32*>(lds)[L_TAG]);
    float y[8];
#pragma unroll
    for (int j = 0; j < 8; ++j) y[j] = 0.f;
    if (g < 28) {
      const int rr = rg < 7 ? rg : 6;
      const u32* xp = reinterpret_cast<const u32*>(lds + L_XN) + s * 28 + rr * 4;
      u32 xa[4], xb[4];
#pragma unroll
      for (int i = 0; i < 4; ++i) { xa[i] = rg < 7 ? xp[i] : 0u; xb[i] = rg < 7 ? xp[224 + i] : 0u; }
#pragma unroll 1
      for (int mat = 0; mat < 2; ++mat) {
        const u32x4* p = reinterpret_cast<const u32x4*>((mat ? wu : wg) + (size_t)(s * 56 + rr * 8) * F + g * 64) + (lane & 7);
        float pa[8], pb[8], r0, r1;
#pragma unroll
        for (int j = 0; j < 8; ++j) { pa[j] = 0.f; pb[j] = 0.f; }
        static_for<0, 2>([&](auto hc) {
          constexpr int hh = decltype(hc)::value;
          u32x4 t[4];
#pragma unroll
          for (int i = 0; i < 4; ++i) t[i] = __builtin_nontemporal_load(p + (hh * 4 + i) * (F / 8));
          gemv_pairs<2, 2 * hh, 4>(t, xa, xb, pa, pb);
          SB();
        });
        reduce_rg(pa, pb, lane, r0, r1);
        gu64* fo = G + G_F + (((g * 2 + half) * 8 + s) * 2 + mat) * 64 + l32;
        put(fo, tag, __float_as_uint(r0));
        put(fo + 32, tag, __float_as_uint(r1));
      }
    }
    const int ib = h * 8 + s;
    if (ib < 112) {
      const int u = lane & 15, col = (ib & 3) * 16 + u;
      const int pos = (col & 1) * 32 + (col >> 3) + ((col >> 2) & 1) * 16 + ((col >> 1) & 1) * 8;
      gu64* fg = G + G_F + ((((ib >> 2) * 2 + b) * 8 + half * 4) * 2 + ((lane >> 4) & 1)) * 64 + pos;
      float sum = 0.f;
      lds_spin(lds, L_FLG + 5, 2u);
#pragma unroll 1
      for (unsigned spin = 0; spin < SPIN_CAP; ++spin) {
        bool ok = true; sum = 0.f;
#pragma unroll
        for (int k = 0; k < 4; ++k) { const u64 v = get(fg + k * 128); sum += __uint_as_float((u32)v); ok &= (u32)(v >> 32) == tag; }
        if (__all(ok)) break;
        __builtin_amdgcn_s_sleep(64);
      }
      sum = rbf(x32sum(sum));
      auto sw = __builtin_amdgcn_permlane16_swap(__float_as_uint(sum), __float_as_uint(sum), false, false);
      const float gv = __uint_as_float(sw[0]), uv = __uint_as_float(sw[1]);
      const float hv = rbf(rbf(gv / (1.f + __expf(-gv))) * uv);
      const u32x4* pd = reinterpret_cast<const u32x4*>(wd + (size_t)(ib * 16) * D) + l56;
#pragma unroll 1
      for (int u4 = 0; u4 < 4; ++u4) {
        u32x4 dr[4];
#pragma unroll
        for (int k = 0; k < 4; ++k) dr[k] = __builtin_nontemporal_load(pd + (u4 * 4 + k) * (D / 8));
#pragma unroll
        for (int k = 0; k < 4; ++k) {
          const float hu = __uint_as_float(__builtin_amdgcn_readlane(__float_as_uint(hv), u4 * 4 + k));
#pragma unroll
          for (int j = 0; j < 4; ++j) { y[2 * j] = fmaf(hu, bflo(dr[k][j]), y[2 * j]); y[2 * j + 1] = fmaf(hu, bfhi(dr[k][j]), y[2 * j + 1]); }
        }
      }
    }
    if (lane < 56) {
      *reinterpret_cast<f32x4*>(lds + L_YF + lane * 8) = f32x4{y[0], y[1], y[2], y[3]};
      *reinterpret_cast<f32x4*>(lds + L_YF + lane * 8 + 4) = f32x4{y[4], y[5], y[6], y[7]};
    }
    {
      const u32x4* wop = reinterpret_cast<const u32x4*>(wo + (size_t)(h * DH + jd * 64) * D) + lane;
      u32x4* dst = reinterpret_cast<u32x4*>(lds + L_WO) + lane;
#pragma unroll 1
      for (int i = 0; i < 56; i += WOB) {
        u32x4 t[WOB];
#pragma unroll
        for (int k = 0; k < WOB; ++k) t[k] = WONT ? __builtin_nontemporal_load(wop + (i + k) * 64) : wop[(i + k) * 64];
#pragma unroll
        for (int k = 0; k < WOB; ++k) dst[(i + k) * 64] = t[k];
      }
    }
    lds_wait();
    if (lane == 0) *(volatile LAS u32*)(lds + L_FLG + 4) = 1u;
    return;
  }

  const int w = wv - 2, tid = threadIdx.x - 128;
  const GAS char* const kbase = (const GAS char*)Kc + (size_t)slot * (1024 * 512) + w * 1024;
  const GAS char* const vbase = (const GAS char*)Vc + (size_t)g * (SK * 512) + (size_t)jk * (4096 * 512) + jd * 128 + w * 1024;
  const u32 koff = lane * 16, voff = ((rg >> 1) * 16 + (rg & 1)) * 512 + (lane & 7) * 16;
  u32x4 buf[RING];
  const GAS char* lp = kbase; u32 loff = koff; int S0 = 8192, SX = 0;
  auto issue = [&](auto jc) {
    constexpr int j = decltype(jc)::value;
    asm volatile("s_waitcnt vmcnt(%0)" :: "i"(INFL - 1) : "memory");
    asm volatile("" : "+s"(lp));
    buf[j] = __builtin_nontemporal_load((const GAS u32x4*)(lp + loff));
    lp += S0; if constexpr (j == 7) lp += SX;
  };
  static_for<0, RING>(issue);
  SB();
  ST(3);
  wg_barrier();
  const u32x4 qp = *reinterpret_cast<const u32x4*>(lds + L_Q + l32 * 4);
  const u32 tag = __builtin_amdgcn_readfirstlane(reinterpret_cast<const u32*>(lds)[L_TAG]);
  ST(4);

  float sr = 0.f;
#pragma unroll 1
  for (int it = 0; it < 4; ++it) {
    if (it == 3) { lp = vbase; loff = voff; S0 = 32768; SX = 524288 - 8 * 32768; }
    const int lj = l32 - (it & 1) * 16;
    static_for<0, RING>([&](auto jc) {
      constexpr int j = decltype(jc)::value;
      const u32x4 kv = buf[j];
      float sacc = dot2(kv[0], qp[0], 0.f);
      sacc = dot2(kv[1], qp[1], sacc); sacc = dot2(kv[2], qp[2], sacc); sacc = dot2(kv[3], qp[3], sacc);
      asm volatile("" : "+v"(sacc));
      issue(jc);
      sacc = sum32(sacc);
      sr = (lj == j) ? sacc : sr;
      SB();
    });
    if (it & 1) {
      lds[L_SC + (it >> 1) * 512 + tid] = sr;
      lds_wait();
      if (lane == 0) __hip_atomic_fetch_add((LAS u32*)(lds + L_FLG + (it >> 1)), 1u, __ATOMIC_RELAXED, __HIP_MEMORY_SCOPE_WORKGROUP);
    }
    if (tid == 0) *(volatile LAS u32*)(lds + L_FLG + 5) = it + 1;
    if (it == 1) ST(6);
    SB();
  }
  ST(5);
  f32x2 ac[4];
#pragma unroll
  for (int j = 0; j < 4; ++j) ac[j] = f32x2{0.f, 0.f};
  const float* pr_base = lds + L_P + w * 512 + ((rg & 1) * 4 + (rg >> 1)) * 8;
  auto vbody = [&](auto refill, const float* pp) {
    f32x4 pr;
    static_for<0, RING>([&](auto jc) {
      constexpr int j = decltype(jc)::value;
      if constexpr ((j & 3) == 0) pr = *reinterpret_cast<const f32x4*>(pp + (j >> 3) * 64 + (j & 7));
      const u32x4 vv = buf[j];
      const float p = pr[j & 3];
#pragma unroll
      for (int k = 0; k < 4; ++k) ac[k] = __builtin_elementwise_fma(f32x2{p, p}, f32x2{bflo(vv[k]), bfhi(vv[k])}, ac[k]);
      asm volatile("" : "+v"(ac[0]), "+v"(ac[1]), "+v"(ac[2]), "+v"(ac[3]));
      if constexpr (decltype(refill)::value) issue(jc);
      SB();
    });
  };
#pragma unroll 1
  for (int it = 0; it < 3; ++it) {
    if ((it & 1) == 0) {
      lds_spin(lds, L_FLG + 2 + (it >> 1), 1u);
      if (it == 2) {
        const float sc = lds[L_TAG + 1];
#pragma unroll
        for (int k = 0; k < 4; ++k) ac[k] *= sc;
      }
    }
    if (it == 2) ST(7);
    lp = vbase + ((it + 1) & 1) * 1048576 + ((it + 1) >> 1) * 262144;
    vbody(IC<1>{}, pr_base + it * 128);
    if (tid == 0) *(volatile LAS u32*)(lds + L_FLG + 5) = it + 5;
  }
  ST(8);
  lds_spin(lds, L_FLG + 4, 1u);
  u32x4 wreg[8];
#pragma unroll
  for (int k = 0; k < 4; ++k) {
    const u32x4 a = *(reinterpret_cast<const u32x4*>(lds + L_WO) + (8 * w + 2 * k) * 56 + l56), c = *(reinterpret_cast<const u32x4*>(lds + L_WO) + (8 * w + 2 * k + 1) * 56 + l56);
    u32x4 lo, hi;
#pragma unroll
    for (int j = 0; j < 4; ++j) { lo[j] = __builtin_amdgcn_perm(c[j], a[j], 0x05040100u); hi[j] = __builtin_amdgcn_perm(c[j], a[j], 0x07060302u); }
    wreg[2 * k] = lo; wreg[2 * k + 1] = hi;
  }
  const float Linv = lds[L_TAG + 2];
  SB();
  vbody(IC<0>{}, pr_base + 3 * 128);
  ST(9);

  {
    float a[8];
#pragma unroll
    for (int j = 0; j < 4; ++j) { a[2 * j] = ac[j][0]; a[2 * j + 1] = ac[j][1]; }
#pragma unroll
    for (int j = 0; j < 8; ++j) { a[j] += dpp<ROR8>(a[j]); a[j] = x32sum(x16sum(a[j])); }
    if (lane < 8) {
      *reinterpret_cast<f32x4*>(lds + L_O + w * 64 + lane * 8) = f32x4{a[0], a[1], a[2], a[3]};
      *reinterpret_cast<f32x4*>(lds + L_O + w * 64 + lane * 8 + 4) = f32x4{a[4], a[5], a[6], a[7]};
    }
    wg_barrier();
    float o = lds[L_O + (lane >> 3) * 64 + 8 * w + (lane & 7)];
    o += dpp<ROR8>(o); o = x32sum(x16sum(o)) * Linv;
    const u32 opk = cvtpk(o, dpp<XOR1>(o));
    float y[8];
#pragma unroll
    for (int j = 0; j < 8; ++j) y[j] = 0.f;
#pragma unroll
    for (int k = 0; k < 4; ++k) {
      const u32 ok = __builtin_amdgcn_readlane(opk, 2 * k);
#pragma unroll
      for (int j = 0; j < 4; ++j) { y[2 * j] = dot2(wreg[2 * k][j], ok, y[2 * j]); y[2 * j + 1] = dot2(wreg[2 * k + 1][j], ok, y[2 * j + 1]); }
    }
    if (lane < 56) {
      *reinterpret_cast<f32x4*>(lds + L_YP + w * D + lane * 8) = f32x4{y[0], y[1], y[2], y[3]};
      *reinterpret_cast<f32x4*>(lds + L_YP + w * D + lane * 8 + 4) = f32x4{y[4], y[5], y[6], y[7]};
    }
    wg_barrier();
    if (tid < D) {
      float t = lds[L_YF + tid];
#pragma unroll
      for (int ww = 0; ww < NW; ++ww) t += lds[L_YP + ww * D + tid];
      put(G + G_Y + ((b * 112 + (tid >> 2)) * 128 + (slot & 127)) * 4 + (tid & 3), tag, __float_as_uint(t));
    }
  }
  ST(10);

  if (bid < 224) {
    const int bc = bid / 112, cg = bid % 112;
    if (w < 2) {
      gu64* src = G + G_Y + (bid * 128 + tid) * 4;
      float v[4] = {0.f, 0.f, 0.f, 0.f};
      bool ok = false;
#pragma unroll 1
      for (unsigned spin = 0; spin < SPIN_CAP; ++spin) {
        if (!ok) {
          ok = true;
#pragma unroll
          for (int k = 0; k < 4; ++k) { const u64 t = get(src + k); v[k] = __uint_as_float((u32)t); ok &= (u32)(t >> 32) == tag; }
        }
        if (__all(ok)) break;
        __builtin_amdgcn_s_sleep(2);
      }
#pragma unroll
      for (int k = 0; k < 4; ++k) v[k] = sum64(v[k]);
      if (lane == 0) *reinterpret_cast<f32x4*>(lds + L_RED + w * 4) = f32x4{v[0], v[1], v[2], v[3]};
    }
    wg_barrier();
    if (tid < 4) {
      const float xv = __uint_as_float((u32)x[bc * D + cg * 4 + tid] << 16);
      const float r = xv + (lds[L_RED + tid] + lds[L_RED + 4 + tid]);
      out[bc * D + cg * 4 + tid] = (u16)(cvtpk(r, r) & 0xffffu);
    }
  } else if (bid == NWG - 1) {
    if (w < 4) {
      bool ok = false;
#pragma unroll 1
      for (unsigned spin = 0; spin < SPIN_CAP; ++spin) {
        if (!ok) ok = (u32)(get(G + G_Y + ((tid >> 7) * 112 * 128 + (tid & 127)) * 4) >> 32) == tag;
        if (__all(ok)) break;
        __builtin_amdgcn_s_sleep(2);
      }
    }
    wg_barrier();
    if (tid == 0) __hip_atomic_store(G + G_E, (u64)tag, RLX_AGENT);
  }
  ST(11);
}

extern "C" void kernel_launch(void* const* d_in, const int* in_sizes, int n_in,
                              void* d_out, int out_size, void* d_ws, size_t ws_size,
                              hipStream_t stream) {
  for (int i = 0; i < n_in; ++i) fprintf(stderr, "in%d=%p ", i, d_in[i]);
  fprintf(stderr, "out=%p ws=%p\n", d_out, d_ws);
  { hipPointerAttribute_t a; if (hipPointerGetAttributes(&a, d_ws) == hipSuccess) fprintf(stderr, "ws type=%d managed=%d flags=%u\n", (int)a.type, a.isManaged, a.allocationFlags); if (hipPointerGetAttributes(&a, d_in[1]) == hipSuccess) fprintf(stderr, "K type=%d managed=%d flags=%u\n", (int)a.type, a.isManaged, a.allocationFlags); }
  main_k<<<NWG, NT, 0, stream>>>((const u16*)d_in[0], (const u32x4*)d_in[1], (const u32x4*)d_in[2], (const u16*)d_in[3],
                                 (const u16*)d_in[4], (const u16*)d_in[6], (const u16*)d_in[7], (const u16*)d_in[8],
                                 (u64*)d_ws, (u16*)d_out);
}
```

```cpp
#include <hip/hip_runtime.h>
#include <hip/hip_bf16.h>
#include <stdint.h>

#define KMODE 0
#define VMODE 0
#define MAPX 1
#define INFL 3
#define WONT 1
#define WOB 4
#define QDELAY 16
#define POLLSLEEP 8

typedef unsigned int u32;
typedef unsigned short u16;
typedef unsigned long long u64;
typedef __attribute__((ext_vector_type(4))) unsigned u32x4;
typedef __attribute__((ext_vector_type(4))) float f32x4;
typedef __attribute__((ext_vector_type(2))) float f32x2;
typedef __attribute__((ext_vector_type(2))) __bf16 bf16x2;
#define GAS __attribute__((address_space(1)))
typedef GAS u32 gu32;
typedef GAS u64 gu64;
#define RLX_AGENT __ATOMIC_RELAXED, __HIP_MEMORY_SCOPE_AGENT

constexpr int D = 448, H = 16, SK = 8192, DH = 256, F = 1792;
constexpr int NWG = 256, NT = 640, NW = 8, RING = 16;
constexpr int NLD = 64;
constexpr unsigned SPIN_CAP = 1u << 18;

constexpr int G_E = 0;
constexpr int G_Q = 32;
constexpr int G_ST = G_Q + 16 * 2 * 4 * 256;
constexpr int G_S = G_ST + 2 * 32 * 16;
constexpr int G_F = G_S + 2 * 256 * 256;
constexpr int G_Y = G_F + 28 * 2 * 8 * 128;

__device__ __forceinline__ float bflo(u32 v) { return __uint_as_float(v << 16); }
__device__ __forceinline__ float bfhi(u32 v) { return __uint_as_float(v & 0xffff0000u); }
__device__ __forceinline__ u32 cvtpk(float lo, float hi) {
  u32 r; asm("v_cvt_pk_bf16_f32 %0, %1, %2" : "=v"(r) : "v"(lo), "v"(hi)); return r;
}
__device__ __forceinline__ float rbf(float f) { return bflo(cvtpk(f, f)); }
__device__ __forceinline__ float dot2(u32 a, u32 b, float c) {
  return __builtin_amdgcn_fdot2_f32_bf16(__builtin_bit_cast(bf16x2, a), __builtin_bit_cast(bf16x2, b), c, false);
}
template <int CTRL> __device__ __forceinline__ float dpp(float x) {
  return __builtin_bit_cast(float, __builtin_amdgcn_mov_dpp(__builtin_bit_cast(int, x), CTRL, 0xf, 0xf, true));
}
constexpr int XOR1 = 0xB1, XOR2 = 0x4E, HMIR = 0x141, MIR = 0x140, ROR8 = 0x128;
__device__ __forceinline__ float sum16(float x) {
  x += dpp<XOR1>(x); x += dpp<XOR2>(x); x += dpp<HMIR>(x); x += dpp<MIR>(x); return x;
}
__device__ __forceinline__ float max16(float x) {
  x = fmaxf(x, dpp<XOR1>(x)); x = fmaxf(x, dpp<XOR2>(x)); x = fmaxf(x, dpp<HMIR>(x)); x = fmaxf(x, dpp<MIR>(x)); return x;
}
__device__ __forceinline__ float x16sum(float x) {
  auto s = __builtin_amdgcn_permlane16_swap(__float_as_uint(x), __float_as_uint(x), false, false);
  return __uint_as_float(s[0]) + __uint_as_float(s[1]);
}
__device__ __forceinline__ float x32sum(float x) {
  auto s = __builtin_amdgcn_permlane32_swap(__float_as_uint(x), __float_as_uint(x), false, false);
  return __uint_as_float(s[0]) + __uint_as_float(s[1]);
}
__device__ __forceinline__ float x16max(float x) {
  auto s = __builtin_amdgcn_permlane16_swap(__float_as_uint(x), __float_as_uint(x), false, false);
  return fmaxf(__uint_as_float(s[0]), __uint_as_float(s[1]));
}
__device__ __forceinline__ float x32max(float x) {
  auto s = __builtin_amdgcn_permlane32_swap(__float_as_uint(x), __float_as_uint(x), false, false);
  return fmaxf(__uint_as_float(s[0]), __uint_as_float(s[1]));
}
__device__ __forceinline__ float sum32(float x) { return x16sum(sum16(x)); }
__device__ __forceinline__ float sum64(float x) { return x32sum(x16sum(sum16(x))); }
__device__ __forceinline__ float max64(float x) { return x32max(x16max(max16(x))); }
#define SB() __builtin_amdgcn_sched_barrier(0)
template <int I> struct IC { static constexpr int value = I; };
template <int B, int E, typename Fn> __device__ __forceinline__ void static_for(Fn&& f) {
  if constexpr (B < E) { f(IC<B>{}); static_for<B + 1, E>(f); }
}
__device__ __forceinline__ void lds_wait() { asm volatile("s_waitcnt lgkmcnt(0)" ::: "memory"); }
__device__ __forceinline__ void wg_barrier() { lds_wait(); __builtin_amdgcn_s_barrier(); asm volatile("" ::: "memory"); }
__device__ __forceinline__ void put(gu64* g, u32 tag, u32 v) { __hip_atomic_store(g, ((u64)tag << 32) | v, RLX_AGENT); }
__device__ __forceinline__ u64 get(gu64* g) { return __hip_atomic_load(g, RLX_AGENT); }

__device__ __forceinline__ float treduce32(float (&pv)[32], int lane) {
#pragma unroll
  for (int j = 0; j < 16; ++j) {
    auto t = __builtin_amdgcn_permlane32_swap(__float_as_uint(pv[j]), __float_as_uint(pv[j + 16]), false, false);
    pv[j] = __uint_as_float(t[0]) + __uint_as_float(t[1]);
  }
#pragma unroll
  for (int j = 0; j < 8; ++j) {
    auto t = __builtin_amdgcn_permlane16_swap(__float_as_uint(pv[j]), __float_as_uint(pv[j + 8]), false, false);
    pv[j] = __uint_as_float(t[0]) + __uint_as_float(t[1]);
  }
  float mine = 0.f;
#pragma unroll
  for (int j = 0; j < 8; ++j) { const float t = sum16(pv[j]); mine = ((lane & 15) == j) ? t : mine; }
  return mine;
}

#define ST(k)

constexpr int L_Q = 0;
constexpr int L_TAG = L_Q + 128;
constexpr int L_FLG = L_TAG + 4;
constexpr int L_XN = L_FLG + 8;
constexpr int L_SC = L_XN + 2 * D;
constexpr int L_P = L_SC + 1024;
constexpr int L_O = L_P + 8 * 512;
constexpr int L_YP = L_O + 8 * 64;
constexpr int L_YF = L_YP + NW * D;
constexpr int L_RED = L_YF + D;
constexpr int L_WO = L_RED + 8;
constexpr int L_END = L_WO + 64 * 224;
constexpr float C2 = 0.0625f * 1.4426950408889634f;
#define LAS __attribute__((address_space(3)))
__device__ __forceinline__ void lds_spin(float* lds, int idx, u32 want) {
  volatile LAS u32* p = (volatile LAS u32*)(lds + idx);
#pragma unroll 1
  for (unsigned spin = 0; spin < (SPIN_CAP << 4); ++spin) {
    if ((u32)__builtin_amdgcn_readfirstlane(*p) >= want) break;
    __builtin_amdgcn_s_sleep(2);
  }
  asm volatile("" ::: "memory");
}
__device__ __forceinline__ u32x4 layernorm8(const u32x4 xr, int lane) {
  float v[8];
#pragma unroll
  for (int j = 0; j < 4; ++j) { v[2 * j] = bflo(xr[j]); v[2 * j + 1] = bfhi(xr[j]); }
  float sm = ((v[0] + v[1]) + (v[2] + v[3])) + ((v[4] + v[5]) + (v[6] + v[7]));
  sm = lane < 56 ? sm : 0.f;
  const float mean = sum64(sm) * (1.0f / D);
  float sq = 0.f;
#pragma unroll
  for (int j = 0; j < 8; ++j) { const float d = v[j] - mean; sq += d * d; }
  sq = lane < 56 ? sq : 0.f;
  const float var = rbf(sum64(sq) * (1.0f / D));
  const float den = rbf(sqrtf(rbf(var + 1e-5f)));
  const float mb = rbf(mean);
  u32x4 o;
#pragma unroll
  for (int j = 0; j < 4; ++j) {
    const u32 d = cvtpk(v[2 * j] - mb, v[2 * j + 1] - mb);
    o[j] = cvtpk(bflo(d) / den, bfhi(d) / den);
  }
  return o;
}
template <int NP, int OFF, int N>
__device__ __forceinline__ void gemv_pairs(const u32x4 (&t)[2 * NP], const u32 (&xa)[N], const u32 (&xb)[N], float (&pa)[8], float (&pb)[8]) {
#pragma unroll
  for (int i = 0; i < NP; ++i) {
    const u32 x0 = xa[OFF + i], x1 = xb[OFF + i];
#pragma unroll
    for (int j = 0; j < 4; ++j) {
      const u32 lo = __builtin_amdgcn_perm(t[2 * i + 1][j], t[2 * i][j], 0x05040100u), hi = __builtin_amdgcn_perm(t[2 * i + 1][j], t[2 * i][j], 0x07060302u);
      pa[2 * j] = dot2(lo, x0, pa[2 * j]); pa[2 * j + 1] = dot2(hi, x0, pa[2 * j + 1]);
      pb[2 * j] = dot2(lo, x1, pb[2 * j]); pb[2 * j + 1] = dot2(hi, x1, pb[2 * j + 1]);
    }
  }
}
__device__ __forceinline__ void reduce_rg(float (&pa)[8], float (&pb)[8], int lane, float& r0, float& r1) {
#pragma unroll
  for (int j = 0; j < 8; ++j) {
    auto t = __builtin_amdgcn_permlane32_swap(__float_as_uint(pa[j]), __float_as_uint(pb[j]), false, false);
    pa[j] = __uint_as_float(t[0]) + __uint_as_float(t[1]);
  }
#pragma unroll
  for (int j = 0; j < 4; ++j) {
    auto t = __builtin_amdgcn_permlane16_swap(__float_as_uint(pa[j]), __float_as_uint(pa[j + 4]), false, false);
    pa[j] = __uint_as_float(t[0]) + __uint_as_float(t[1]);
  }
  float a0 = pa[0] + dpp<ROR8>(pa[0]), a1 = pa[1] + dpp<ROR8>(pa[1]), a2 = pa[2] + dpp<ROR8>(pa[2]), a3 = pa[3] + dpp<ROR8>(pa[3]);
  asm volatile("" : "+v"(a0), "+v"(a1), "+v"(a2), "+v"(a3));
  const bool bit3 = (lane >> 3) & 1;
  r0 = bit3 ? a2 : a0; r1 = bit3 ? a3 : a1;
}

__global__ void __launch_bounds__(NT) main_k(const u16* __restrict__ x, const u32x4* __restrict__ Kc, const u32x4* __restrict__ Vc,
                                            const u16* __restrict__ wq, const u16* __restrict__ wo, const u16* __restrict__ wg,
                                            const u16* __restrict__ wu, const u16* __restrict__ wd, u64* ws, u16* __restrict__ out) {
  __shared__ __attribute__((aligned(16))) float lds[L_END];
  gu64* const G = (gu64*)ws;
  const int lane = threadIdx.x & 63, half = lane >> 5, l32 = lane & 31;
  const int wv = __builtin_amdgcn_readfirstlane(threadIdx.x >> 6);
  const int bid = blockIdx.x;
  int b, h, s;
  if (MAPX == 0) { b = bid >> 7; h = (bid >> 3) & 15; s = bid & 7; }
  else { const int xcd = bid & 7, idx = bid >> 3; h = xcd * 2 + (idx >> 4); b = (idx >> 3) & 1; s = idx & 7; }
  const int g = b * H + h, slot = g * 8 + s;
  const int l56 = lane < 56 ? lane : 55;
  const int jd = s & 3, jk = s >> 2, rg = lane >> 3;

  if (wv == 0) {
    ST(0);
    __builtin_amdgcn_s_setprio(3);
    const int j16 = b * 8 + s, qc = j16 & 3, qk = j16 >> 2;
    const u32 epoch = __hip_atomic_load((gu32*)(G + G_E), RLX_AGENT);
    const u32x4 xr0 = *reinterpret_cast<const u32x4*>(x + l56 * 8);
    const u32x4 xr1 = *reinterpret_cast<const u32x4*>(x + D + l56 * 8);
    u32x4 qw[14];
    const u32x4* p = reinterpret_cast<const u32x4*>(wq + (size_t)(qk * 112 + rg * 14) * (H * DH) + h * DH + qc * 64) + (lane & 7);
#pragma unroll
    for (int i = 0; i < 14; ++i) qw[i] = __builtin_nontemporal_load(p + i * (H * DH / 8));
    SB();
    if (lane < 8) reinterpret_cast<u32*>(lds)[L_FLG + lane] = 0u;
    {
      const u32x4 n0 = layernorm8(xr0, lane), n1 = layernorm8(xr1, lane);
      if (lane < 56) { *reinterpret_cast<u32x4*>(lds + L_XN + lane * 4) = n0; *reinterpret_cast<u32x4*>(lds + L_XN + 224 + lane * 4) = n1; }
    }
    lds_wait();
    const u32 tag = __builtin_amdgcn_readfirstlane(epoch) + 0x9E3779B9u;
    {
      float pa[8], pb[8], r0, r1;
#pragma unroll
      for (int j = 0; j < 8; ++j) { pa[j] = 0.f; pb[j] = 0.f; }
      const u32* xp = reinterpret_cast<const u32*>(lds + L_XN) + qk * 56 + rg * 7;
      u32 xa[7], xb[7];
#pragma unroll
      for (int i = 0; i < 7; ++i) { xa[i] = xp[i]; xb[i] = xp[224 + i]; }
      gemv_pairs<7, 0, 7>(qw, xa, xb, pa, pb);
      reduce_rg(pa, pb, lane, r0, r1);
      gu64* qo = G + G_Q + ((h * 2 + half) * 4 + qk) * 256 + qc * 64 + l32;
      put(qo, tag, __float_as_uint(r0));
      put(qo + 32, tag, __float_as_uint(r1));
    }
    __builtin_amdgcn_s_sleep(QDELAY);
    ST(1);
    {
      gu64* qg = G + G_Q + (h * 2 + b) * 1024 + half * 128 + l32;
      float qs[4];
#pragma unroll 1
      for (unsigned spin = 0; spin < SPIN_CAP; ++spin) {
        u64 v[16];
#pragma unroll
        for (int k = 0; k < 4; ++k)
#pragma unroll
          for (int j = 0; j < 4; ++j) v[k * 4 + j] = get(qg + k * 256 + j * 32);
        bool ok = true;
#pragma unroll
        for (int k = 0; k < 16; ++k) ok &= (u32)(v[k] >> 32) == tag;
#pragma unroll
        for (int j = 0; j < 4; ++j) qs[j] = (__uint_as_float((u32)v[j]) + __uint_as_float((u32)v[4 + j])) + (__uint_as_float((u32)v[8 + j]) + __uint_as_float((u32)v[12 + j]));
        if (__all(ok)) break;
      }
      const int pr = (l32 & 7) * 4 + ((l32 >> 4) & 1) * 2 + ((l32 >> 3) & 1);
      reinterpret_cast<u32*>(lds + L_Q)[(half * 2) * 32 + pr] = cvtpk(qs[0], qs[1]);
      reinterpret_cast<u32*>(lds + L_Q)[(half * 2 + 1) * 32 + pr] = cvtpk(qs[2], qs[3]);
      if (lane == 0) reinterpret_cast<u32*>(lds)[L_TAG] = tag;
    }
    ST(2);
    wg_barrier();
    float mA = 0.f, M1 = 0.f;
#pragma unroll 1
    for (int st = 0; st < 2; ++st) {
      lds_spin(lds, L_FLG + st, 8u);
      {
        u32 sp[4]; float mx = -INFINITY;
#pragma unroll
        for (int k = 0; k < 4; ++k) {
          sp[k] = cvtpk(lds[L_SC + st * 512 + (2 * k) * 64 + lane], lds[L_SC + st * 512 + (2 * k + 1) * 64 + lane]);
          put(G + G_S + ((st * 256 + slot) * 4 + k) * 64 + lane, tag, sp[k]);
          mx = fmaxf(mx, fmaxf(bflo(sp[k]), bfhi(sp[k])));
        }
        mx = max64(mx) * C2;
        float ls = 0.f;
#pragma unroll
        for (int k = 0; k < 4; ++k) ls += __builtin_amdgcn_exp2f(bflo(sp[k]) * C2 - mx) + __builtin_amdgcn_exp2f(bfhi(sp[k]) * C2 - mx);
        ls = sum64(ls);
        if (lane < 2) put(G + G_ST + (st * 32 + g) * 16 + s * 2 + lane, tag, __float_as_uint(lane ? ls : mx));
      }
      lds_spin(lds, L_FLG + 5, 3u + 2u * st);
      gu64* tg = G + G_ST + (st * 32 + g) * 16 + (lane & 15);
      gu64* sg = G + G_S + (st * 256 + g * 8 + jk * 4) * 256 + lane;
      float sv = 0.f;
      u32 sc[16];
#pragma unroll 1
      for (unsigned spin = 0; spin < SPIN_CAP; ++spin) {
        const u64 a = get(tg);
        sv = __uint_as_float((u32)a);
        bool ok = (u32)(a >> 32) == tag;
#pragma unroll
        for (int k = 0; k < 16; ++k) { const u64 v = get(sg + k * 64); sc[k] = (u32)v; ok &= (u32)(v >> 32) == tag; }
        if (__all(ok)) break;
        __builtin_amdgcn_s_sleep(POLLSLEEP);
      }
      const bool isl = lane & 1;
      float M = max16(isl ? -INFINITY : sv);
      if (st == 0) { mA = sv; M1 = M; }
      else {
        M = fmaxf(M, M1);
        const float ea = __builtin_amdgcn_exp2f(dpp<XOR1>(mA) - M) * mA, eb = __builtin_amdgcn_exp2f(dpp<XOR1>(sv) - M) * sv;
        const float Li = 1.0f / sum16(isl ? ea + eb : 0.f);
        if (lane == 0) { lds[L_TAG + 1] = __builtin_amdgcn_exp2f(M1 - M); lds[L_TAG + 2] = Li; }
      }
      float* pw = lds + L_P + st * 256 + (half * 4 + (l32 & 3)) * 8 + (l32 >> 2);
#pragma unroll
      for (int k = 0; k < 16; ++k) {
        pw[(2 * (k & 3)) * 512 + (k >> 2) * 64] = __builtin_amdgcn_exp2f(bflo(sc[k]) * C2 - M);
        pw[(2 * (k & 3) + 1) * 512 + (k >> 2) * 64] = __builtin_amdgcn_exp2f(bfhi(sc[k]) * C2 - M);
      }
      lds_wait();
      if (lane == 0) *(volatile LAS u32*)(lds + L_FLG + 2 + st) = 1u;
    }
    return;
  }
  if (wv == 1) {
    wg_barrier();
    const u32 tag = __builtin_amdgcn_readfirstlane(reinterpret_cast<const u32*>(lds)[L_TAG]);
    float y[8];
#pragma unroll
    for (int j = 0; j < 8; ++j) y[j] = 0.f;
    if (g < 28) {
      const int rr = rg < 7 ? rg : 6;
      const u32* xp = reinterpret_cast<const u32*>(lds + L_XN) + s * 28 + rr * 4;
      u32 xa[4], xb[4];
#pragma unroll
      for (int i = 0; i < 4; ++i) { xa[i] = rg < 7 ? xp[i] : 0u; xb[i] = rg < 7 ? xp[224 + i] : 0u; }
#pragma unroll 1
      for (int mat = 0; mat < 2; ++mat) {
        const u32x4* p = reinterpret_cast<const u32x4*>((mat ? wu : wg) + (size_t)(s * 56 + rr * 8) * F + g * 64) + (lane & 7);
        float pa[8], pb[8], r0, r1;
#pragma unroll
        for (int j = 0; j < 8; ++j) { pa[j] = 0.f; pb[j] = 0.f; }
        static_for<0, 2>([&](auto hc) {
          constexpr int hh = decltype(hc)::value;
          u32x4 t[4];
#pragma unroll
          for (int i = 0; i < 4; ++i) t[i] = __builtin_nontemporal_load(p + (hh * 4 + i) * (F / 8));
          gemv_pairs<2, 2 * hh, 4>(t, xa, xb, pa, pb);
          SB();
        });
        reduce_rg(pa, pb, lane, r0, r1);
        gu64* fo = G + G_F + (((g * 2 + half) * 8 + s) * 2 + mat) * 64 + l32;
        put(fo, tag, __float_as_uint(r0));
        put(fo + 32, tag, __float_as_uint(r1));
      }
    }
    const int ib = h * 8 + s;
    if (ib < 112) {
      const int u = lane & 15, col = (ib & 3) * 16 + u;
      const int pos = (col & 1) * 32 + (col >> 3) + ((col >> 2) & 1) * 16 + ((col >> 1) & 1) * 8;
      gu64* fg = G + G_F + ((((ib >> 2) * 2 + b) * 8 + half * 4) * 2 + ((lane >> 4) & 1)) * 64 + pos;
      float sum = 0.f;
      lds_spin(lds, L_FLG + 5, 2u);
#pragma unroll 1
      for (unsigned spin = 0; spin < SPIN_CAP; ++spin) {
        bool ok = true; sum = 0.f;
#pragma unroll
        for (int k = 0; k < 4; ++k) { const u64 v = get(fg + k * 128); sum += __uint_as_float((u32)v); ok &= (u32)(v >> 32) == tag; }
        if (__all(ok)) break;
        __builtin_amdgcn_s_sleep(64);
      }
      sum = rbf(x32sum(sum));
      auto sw = __builtin_amdgcn_permlane16_swap(__float_as_uint(sum), __float_as_uint(sum), false, false);
      const float gv = __uint_as_float(sw[0]), uv = __uint_as_float(sw[1]);
      const float hv = rbf(rbf(gv / (1.f + __expf(-gv))) * uv);
      const u32x4* pd = reinterpret_cast<const u32x4*>(wd + (size_t)(ib * 16) * D) + l56;
#pragma unroll 1
      for (int u4 = 0; u4 < 4; ++u4) {
        u32x4 dr[4];
#pragma unroll
        for (int k = 0; k < 4; ++k) dr[k] = __builtin_nontemporal_load(pd + (u4 * 4 + k) * (D / 8));
#pragma unroll
        for (int k = 0; k < 4; ++k) {
          const float hu = __uint_as_float(__builtin_amdgcn_readlane(__float_as_uint(hv), u4 * 4 + k));
#pragma unroll
          for (int j = 0; j < 4; ++j) { y[2 * j] = fmaf(hu, bflo(dr[k][j]), y[2 * j]); y[2 * j + 1] = fmaf(hu, bfhi(dr[k][j]), y[2 * j + 1]); }
        }
      }
    }
    if (lane < 56) {
      *reinterpret_cast<f32x4*>(lds + L_YF + lane * 8) = f32x4{y[0], y[1], y[2], y[3]};
      *reinterpret_cast<f32x4*>(lds + L_YF + lane * 8 + 4) = f32x4{y[4], y[5], y[6], y[7]};
    }
    {
      const u32x4* wop = reinterpret_cast<const u32x4*>(wo + (size_t)(h * DH + jd * 64) * D) + lane;
      u32x4* dst = reinterpret_cast<u32x4*>(lds + L_WO) + lane;
#pragma unroll 1
      for (int i = 0; i < 56; i += WOB) {
        u32x4 t[WOB];
#pragma unroll
        for (int k = 0; k < WOB; ++k) t[k] = WONT ? __builtin_nontemporal_load(wop + (i + k) * 64) : wop[(i + k) * 64];
#pragma unroll
        for (int k = 0; k < WOB; ++k) dst[(i + k) * 64] = t[k];
      }
    }
    lds_wait();
    if (lane == 0) *(volatile LAS u32*)(lds + L_FLG + 4) = 1u;
    return;
  }

  const int w = wv - 2, tid = threadIdx.x - 128;
  const GAS char* const kbase = (const GAS char*)Kc + (size_t)slot * (1024 * 512) + w * 1024;
  const GAS char* const vbase = (const GAS char*)Vc + (size_t)g * (SK * 512) + (size_t)jk * (4096 * 512) + jd * 128 + w * 1024;
  const u32 koff = lane * 16, voff = ((rg >> 1) * 16 + (rg & 1)) * 512 + (lane & 7) * 16;
  u32x4 buf[RING];
  const GAS char* lp = kbase; u32 loff = koff; int S0 = 8192, SX = 0;
  auto issue = [&](auto jc) {
    constexpr int j = decltype(jc)::value;
    asm volatile("s_waitcnt vmcnt(%0)" :: "i"(INFL - 1) : "memory");
    asm volatile("" : "+s"(lp));
    buf[j] = __builtin_nontemporal_load((const GAS u32x4*)(lp + loff));
    lp += S0; if constexpr (j == 7) lp += SX;
  };
  static_for<0, RING>(issue);
  SB();
  ST(3);
  wg_barrier();
  const u32x4 qp = *reinterpret_cast<const u32x4*>(lds + L_Q + l32 * 4);
  const u32 tag = __builtin_amdgcn_readfirstlane(reinterpret_cast<const u32*>(lds)[L_TAG]);
  ST(4);

  float sr = 0.f;
#pragma unroll 1
  for (int it = 0; it < 4; ++it) {
    if (it == 3) { lp = vbase; loff = voff; S0 = 32768; SX = 524288 - 8 * 32768; }
    const int lj = l32 - (it & 1) * 16;
    static_for<0, RING>([&](auto jc) {
      constexpr int j = decltype(jc)::value;
      const u32x4 kv = buf[j];
      float sacc = dot2(kv[0], qp[0], 0.f);
      sacc = dot2(kv[1], qp[1], sacc); sacc = dot2(kv[2], qp[2], sacc); sacc = dot2(kv[3], qp[3], sacc);
      asm volatile("" : "+v"(sacc));
      issue(jc);
      sacc = sum32(sacc);
      sr = (lj == j) ? sacc : sr;
      SB();
    });
    if (it & 1) {
      lds[L_SC + (it >> 1) * 512 + tid] = sr;
      lds_wait();
      if (lane == 0) __hip_atomic_fetch_add((LAS u32*)(lds + L_FLG + (it >> 1)), 1u, __ATOMIC_RELAXED, __HIP_MEMORY_SCOPE_WORKGROUP);
    }
    if (tid == 0) *(volatile LAS u32*)(lds + L_FLG + 5) = it + 1;
    if (it == 1) ST(6);
    SB();
  }
  ST(5);
  f32x2 ac[4];
#pragma unroll
  for (int j = 0; j < 4; ++j) ac[j] = f32x2{0.f, 0.f};
  const float* pr_base = lds + L_P + w * 512 + ((rg & 1) * 4 + (rg >> 1)) * 8;
  auto vbody = [&](auto refill, const float* pp) {
    f32x4 pr;
    static_for<0, RING>([&](auto jc) {
      constexpr int j = decltype(jc)::value;
      if constexpr ((j & 3) == 0) pr = *reinterpret_cast<const f32x4*>(pp + (j >> 3) * 64 + (j & 7));
      const u32x4 vv = buf[j];
      const float p = pr[j & 3];
#pragma unroll
      for (int k = 0; k < 4; ++k) ac[k] = __builtin_elementwise_fma(f32x2{p, p}, f32x2{bflo(vv[k]), bfhi(vv[k])}, ac[k]);
      asm volatile("" : "+v"(ac[0]), "+v"(ac[1]), "+v"(ac[2]), "+v"(ac[3]));
      if constexpr (decltype(refill)::value) issue(jc);
      SB();
    });
  };
#pragma unroll 1
  for (int it = 0; it < 3; ++it) {
    if ((it & 1) == 0) {
      lds_spin(lds, L_FLG + 2 + (it >> 1), 1u);
      if (it == 2) {
        const float sc = lds[L_TAG + 1];
#pragma unroll
        for (int k = 0; k < 4; ++k) ac[k] *= sc;
      }
    }
    if (it == 2) ST(7);
    lp = vbase + ((it + 1) & 1) * 1048576 + ((it + 1) >> 1) * 262144;
    vbody(IC<1>{}, pr_base + it * 128);
    if (tid == 0) *(volatile LAS u32*)(lds + L_FLG + 5) = it + 5;
  }
  ST(8);
  lds_spin(lds, L_FLG + 4, 1u);
  u32x4 wreg[8];
#pragma unroll
  for (int k = 0; k < 4; ++k) {
    const u32x4 a = *(reinterpret_cast<const u32x4*>(lds + L_WO) + (8 * w + 2 * k) * 56 + l56), c = *(reinterpret_cast<const u32x4*>(lds + L_WO) + (8 * w + 2 * k + 1) * 56 + l56);
    u32x4 lo, hi;
#pragma unroll
    for (int j = 0; j < 4; ++j) { lo[j] = __builtin_amdgcn_perm(c[j], a[j], 0x05040100u); hi[j] = __builtin_amdgcn_perm(c[j], a[j], 0x07060302u); }
    wreg[2 * k] = lo; wreg[2 * k + 1] = hi;
  }
  const float Linv = lds[L_TAG + 2];
  SB();
  vbody(IC<0>{}, pr_base + 3 * 128);
  ST(9);

  {
    float a[8];
#pragma unroll
    for (int j = 0; j < 4; ++j) { a[2 * j] = ac[j][0]; a[2 * j + 1] = ac[j][1]; }
#pragma unroll
    for (int j = 0; j < 8; ++j) { a[j] += dpp<ROR8>(a[j]); a[j] = x32sum(x16sum(a[j])); }
    if (lane < 8) {
      *reinterpret_cast<f32x4*>(lds + L_O + w * 64 + lane * 8) = f32x4{a[0], a[1], a[2], a[3]};
      *reinterpret_cast<f32x4*>(lds + L_O + w * 64 + lane * 8 + 4) = f32x4{a[4], a[5], a[6], a[7]};
    }
    wg_barrier();
    float o = lds[L_O + (lane >> 3) * 64 + 8 * w + (lane & 7)];
    o += dpp<ROR8>(o); o = x32sum(x16sum(o)) * Linv;
    const u32 opk = cvtpk(o, dpp<XOR1>(o));
    float y[8];
#pragma unroll
    for (int j = 0; j < 8; ++j) y[j] = 0.f;
#pragma unroll
    for (int k = 0; k < 4; ++k) {
      const u32 ok = __builtin_amdgcn_readlane(opk, 2 * k);
#pragma unroll
      for (int j = 0; j < 4; ++j) { y[2 * j] = dot2(wreg[2 * k][j], ok, y[2 * j]); y[2 * j + 1] = dot2(wreg[2 * k + 1][j], ok, y[2 * j + 1]); }
    }
    if (lane < 56) {
      *reinterpret_cast<f32x4*>(lds + L_YP + w * D + lane * 8) = f32x4{y[0], y[1], y[2], y[3]};
      *reinterpret_cast<f32x4*>(lds + L_YP + w * D + lane * 8 + 4) = f32x4{y[4], y[5], y[6], y[7]};
    }
    wg_barrier();
    if (tid < D) {
      float t = lds[L_YF + tid];
#pragma unroll
      for (int ww = 0; ww < NW; ++ww) t += lds[L_YP + ww * D + tid];
      put(G + G_Y + ((b * 112 + (tid >> 2)) * 128 + (slot & 127)) * 4 + (tid & 3), tag, __float_as_uint(t));
    }
  }
  ST(10);

  if (bid < 224) {
    const int bc = bid / 112, cg = bid % 112;
    if (w < 2) {
      gu64* src = G + G_Y + (bid * 128 + tid) * 4;
      float v[4] = {0.f, 0.f, 0.f, 0.f};
      bool ok = false;
#pragma unroll 1
      for (unsigned spin = 0; spin < SPIN_CAP; ++spin) {
        if (!ok) {
          ok = true;
#pragma unroll
          for (int k = 0; k < 4; ++k) { const u64 t = get(src + k); v[k] = __uint_as_float((u32)t); ok &= (u32)(t >> 32) == tag; }
        }
        if (__all(ok)) break;
        __builtin_amdgcn_s_sleep(2);
      }
#pragma unroll
      for (int k = 0; k < 4; ++k) v[k] = sum64(v[k]);
      if (lane == 0) *reinterpret_cast<f32x4*>(lds + L_RED + w * 4) = f32x4{v[0], v[1], v[2], v[3]};
    }
    wg_barrier();
    if (tid < 4) {
      const float xv = __uint_as_float((u32)x[bc * D + cg * 4 + tid] << 16);
      const float r = xv + (lds[L_RED + tid] + lds[L_RED + 4 + tid]);
      out[bc * D + cg * 4 + tid] = (u16)(cvtpk(r, r) & 0xffffu);
    }
  } else if (bid == NWG - 1) {
    if (w < 4) {
      bool ok = false;
#pragma unroll 1
      for (unsigned spin = 0; spin < SPIN_CAP; ++spin) {
        if (!ok) ok = (u32)(get(G + G_Y + ((tid >> 7) * 112 * 128 + (tid & 127)) * 4) >> 32) == tag;
        if (__all(ok)) break;
        __builtin_amdgcn_s_sleep(2);
      }
    }
    wg_barrier();
    if (tid == 0) __hip_atomic_store(G + G_E, (u64)tag, RLX_AGENT);
  }
  ST(11);
}

__global__ void __launch_bounds__(640) empty_big(u32* out) {
  __shared__ float big[25000];
  big[threadIdx.x] = threadIdx.x;
  __syncthreads();
  if (big[(threadIdx.x + 1) % 640] == -1.f) out[0] = 1;
}
__global__ void __launch_bounds__(256) empty_small(u32* out) { if (out == nullptr) out[0] = 1; }
extern "C" void kernel_launch(void* const* d_in, const int* in_sizes, int n_in,
                              void* d_out, int out_size, void* d_ws, size_t ws_size,
                              hipStream_t stream) {

  main_k<<<NWG, NT, 0, stream>>>((const u16*)d_in[0], (const u32x4*)d_in[1], (const u32x4*)d_in[2], (const u16*)d_in[3],
                                 (const u16*)d_in[4], (const u16*)d_in[6], (const u16*)d_in[7], (const u16*)d_in[8],
                                 (u64*)d_ws, (u16*)d_out);
  empty_big<<<256, 640, 0, stream>>>((u32*)d_ws + (100 << 20) / 4);
}
```

```cpp
#include <hip/hip_runtime.h>
#include <hip/hip_bf16.h>
#include <stdint.h>

#define MAPX 0
#define NW 8
#define INFL (NW == 4 ? 8 : 3)
#define QDELAY 16
#define GOFF 0

typedef unsigned int u32;
typedef unsigned short u16;
typedef unsigned long long u64;
typedef __attribute__((ext_vector_type(4))) unsigned u32x4;
typedef __attribute__((ext_vector_type(4))) float f32x4;
typedef __attribute__((ext_vector_type(2))) float f32x2;
typedef __attribute__((ext_vector_type(2))) __bf16 bf16x2;
#define GAS __attribute__((address_space(1)))
#define LAS __attribute__((address_space(3)))
typedef GAS u32 gu32;
typedef GAS u64 gu64;
#define RLX_AGENT __ATOMIC_RELAXED, __HIP_MEMORY_SCOPE_AGENT

constexpr int D = 448, H = 16, SK = 8192, DH = 256, F = 1792;
constexpr int NWG = 256, NT = (NW + 1) * 64, RING = 128 / NW, NLD = 512 / NW;
constexpr unsigned SPIN_CAP = 1u << 18;
constexpr float C2 = 0.0625f * 1.4426950408889634f;

constexpr int G_E = 0;
constexpr int G_E2 = G_E + 16 * 16;
constexpr int G_Q = G_E2 + 16;
constexpr int G_Y = G_Q + 16 * 2 * 4 * 256;
constexpr int G_P = G_Y + 2 * 112 * 256 * 4;
constexpr int P_A = 0, P_ML = P_A + 256 * 256, P_F = P_ML + 512;

__device__ __forceinline__ float bflo(u32 v) { return __uint_as_float(v << 16); }
__device__ __forceinline__ float bfhi(u32 v) { return __uint_as_float(v & 0xffff0000u); }
__device__ __forceinline__ u32 cvtpk(float lo, float hi) {
  u32 r; asm("v_cvt_pk_bf16_f32 %0, %1, %2" : "=v"(r) : "v"(lo), "v"(hi)); return r;
}
__device__ __forceinline__ float rbf(float f) { return bflo(cvtpk(f, f)); }
__device__ __forceinline__ float dot2(u32 a, u32 b, float c) {
  return __builtin_amdgcn_fdot2_f32_bf16(__builtin_bit_cast(bf16x2, a), __builtin_bit_cast(bf16x2, b), c, false);
}
template <int CTRL> __device__ __forceinline__ float dpp(float x) {
  return __builtin_bit_cast(float, __builtin_amdgcn_mov_dpp(__builtin_bit_cast(int, x), CTRL, 0xf, 0xf, true));
}
constexpr int XOR1 = 0xB1, XOR2 = 0x4E, HMIR = 0x141, MIR = 0x140, ROR8 = 0x128;
__device__ __forceinline__ float sum16(float x) {
  x += dpp<XOR1>(x); x += dpp<XOR2>(x); x += dpp<HMIR>(x); x += dpp<MIR>(x); return x;
}
__device__ __forceinline__ float max16(float x) {
  x = fmaxf(x, dpp<XOR1>(x)); x = fmaxf(x, dpp<XOR2>(x)); x = fmaxf(x, dpp<HMIR>(x)); x = fmaxf(x, dpp<MIR>(x)); return x;
}
__device__ __forceinline__ float x16sum(float x) {
  auto s = __builtin_amdgcn_permlane16_swap(__float_as_uint(x), __float_as_uint(x), false, false);
  return __uint_as_float(s[0]) + __uint_as_float(s[1]);
}
__device__ __forceinline__ float x32sum(float x) {
  auto s = __builtin_amdgcn_permlane32_swap(__float_as_uint(x), __float_as_uint(x), false, false);
  return __uint_as_float(s[0]) + __uint_as_float(s[1]);
}
__device__ __forceinline__ float x16max(float x) {
  auto s = __builtin_amdgcn_permlane16_swap(__float_as_uint(x), __float_as_uint(x), false, false);
  return fmaxf(__uint_as_float(s[0]), __uint_as_float(s[1]));
}
__device__ __forceinline__ float x32max(float x) {
  auto s = __builtin_amdgcn_permlane32_swap(__float_as_uint(x), __float_as_uint(x), false, false);
  return fmaxf(__uint_as_float(s[0]), __uint_as_float(s[1]));
}
__device__ __forceinline__ float sum32(float x) { return x16sum(sum16(x)); }
__device__ __forceinline__ float sum64(float x) { return x32sum(x16sum(sum16(x))); }
__device__ __forceinline__ float max64(float x) { return x32max(x16max(max16(x))); }
#define SB() __builtin_amdgcn_sched_barrier(0)
template <int I> struct IC { static constexpr int value = I; };
template <int B, int E, typename Fn> __device__ __forceinline__ void static_for(Fn&& f) {
  if constexpr (B < E) { f(IC<B>{}); static_for<B + 1, E>(f); }
}
__device__ __forceinline__ void lds_wait() { asm volatile("s_waitcnt lgkmcnt(0)" ::: "memory"); }
__device__ __forceinline__ void wg_barrier() { lds_wait(); __builtin_amdgcn_s_barrier(); asm volatile("" ::: "memory"); }
__device__ __forceinline__ void put(gu64* g, u32 tag, u32 v) { __hip_atomic_store(g, ((u64)tag << 32) | v, RLX_AGENT); }
__device__ __forceinline__ u64 get(gu64* g) { return __hip_atomic_load(g, RLX_AGENT); }

__device__ __forceinline__ float treduce32(float (&pv)[32], int lane) {
#pragma unroll
  for (int j = 0; j < 16; ++j) {
    auto t = __builtin_amdgcn_permlane32_swap(__float_as_uint(pv[j]), __float_as_uint(pv[j + 16]), false, false);
    pv[j] = __uint_as_float(t[0]) + __uint_as_float(t[1]);
  }
#pragma unroll
  for (int j = 0; j < 8; ++j) {
    auto t = __builtin_amdgcn_permlane16_swap(__float_as_uint(pv[j]), __float_as_uint(pv[j + 8]), false, false);
    pv[j] = __uint_as_float(t[0]) + __uint_as_float(t[1]);
  }
  float mine = 0.f;
#pragma unroll
  for (int j = 0; j < 8; ++j) { const float t = sum16(pv[j]); mine = ((lane & 15) == j) ? t : mine; }
  return mine;
}

#define ST(k)

constexpr int L_Q = 0;
constexpr int L_XN = L_Q + 128;
constexpr int L_P = L_XN + 448;
constexpr int L_O = L_P + 1024;
constexpr int L_ML = L_O + NW * 256;
constexpr int L_END = L_ML + 2 * NW;

__device__ __forceinline__ u32x4 layernorm8(const u32x4 xr, int lane) {
  float v[8];
#pragma unroll
  for (int j = 0; j < 4; ++j) { v[2 * j] = bflo(xr[j]); v[2 * j + 1] = bfhi(xr[j]); }
  float sm = ((v[0] + v[1]) + (v[2] + v[3])) + ((v[4] + v[5]) + (v[6] + v[7]));
  sm = lane < 56 ? sm : 0.f;
  const float mean = sum64(sm) * (1.0f / D);
  float sq = 0.f;
#pragma unroll
  for (int j = 0; j < 8; ++j) { const float d = v[j] - mean; sq += d * d; }
  sq = lane < 56 ? sq : 0.f;
  const float var = rbf(sum64(sq) * (1.0f / D));
  const float den = rbf(sqrtf(rbf(var + 1e-5f)));
  const float mb = rbf(mean);
  u32x4 o;
#pragma unroll
  for (int j = 0; j < 4; ++j) {
    const u32 d = cvtpk(v[2 * j] - mb, v[2 * j + 1] - mb);
    o[j] = cvtpk(bflo(d) / den, bfhi(d) / den);
  }
  return o;
}
template <int NP, int OFF, int N>
__device__ __forceinline__ void gemv_pairs(const u32x4 (&t)[2 * NP], const u32 (&xa)[N], const u32 (&xb)[N], float (&pa)[8], float (&pb)[8]) {
#pragma unroll
  for (int i = 0; i < NP; ++i) {
    const u32 x0 = xa[OFF + i], x1 = xb[OFF + i];
#pragma unroll
    for (int j = 0; j < 4; ++j) {
      const u32 lo = __builtin_amdgcn_perm(t[2 * i + 1][j], t[2 * i][j], 0x05040100u), hi = __builtin_amdgcn_perm(t[2 * i + 1][j], t[2 * i][j], 0x07060302u);
      pa[2 * j] = dot2(lo, x0, pa[2 * j]); pa[2 * j + 1] = dot2(hi, x0, pa[2 * j + 1]);
      pb[2 * j] = dot2(lo, x1, pb[2 * j]); pb[2 * j + 1] = dot2(hi, x1, pb[2 * j + 1]);
    }
  }
}
__device__ __forceinline__ void reduce_rg(float (&pa)[8], float (&pb)[8], int lane, float& r0, float& r1) {
#pragma unroll
  for (int j = 0; j < 8; ++j) {
    auto t = __builtin_amdgcn_permlane32_swap(__float_as_uint(pa[j]), __float_as_uint(pb[j]), false, false);
    pa[j] = __uint_as_float(t[0]) + __uint_as_float(t[1]);
  }
#pragma unroll
  for (int j = 0; j < 4; ++j) {
    auto t = __builtin_amdgcn_permlane16_swap(__float_as_uint(pa[j]), __float_as_uint(pa[j + 4]), false, false);
    pa[j] = __uint_as_float(t[0]) + __uint_as_float(t[1]);
  }
  float a0 = pa[0] + dpp<ROR8>(pa[0]), a1 = pa[1] + dpp<ROR8>(pa[1]), a2 = pa[2] + dpp<ROR8>(pa[2]), a3 = pa[3] + dpp<ROR8>(pa[3]);
  asm volatile("" : "+v"(a0), "+v"(a1), "+v"(a2), "+v"(a3));
  const bool bit3 = (lane >> 3) & 1;
  r0 = bit3 ? a2 : a0; r1 = bit3 ? a3 : a1;
}

__global__ void __launch_bounds__(NT) main_k(const u16* __restrict__ x, const u32x4* __restrict__ Kc, const u32x4* __restrict__ Vc,
                                            const u16* __restrict__ wq, const u16* __restrict__ wg, const u16* __restrict__ wu, u64* ws) {
  __shared__ __attribute__((aligned(16))) float lds[L_END];
  gu64* const G = (gu64*)ws + (size_t)GOFF * 131072;
  float* const P = reinterpret_cast<float*>(ws + (size_t)GOFF * 131072 + G_P);
  const int lane = threadIdx.x & 63, half = lane >> 5, l32 = lane & 31;
  const int wv = __builtin_amdgcn_readfirstlane(threadIdx.x >> 6);
  const int bid = blockIdx.x;
  int b, h, s;
  if (MAPX == 0) { b = bid >> 7; h = (bid >> 3) & 15; s = bid & 7; }
  else if (MAPX == 1) { const int xcd = bid & 7, idx = bid >> 3; h = xcd * 2 + (idx >> 4); b = (idx >> 3) & 1; s = idx & 7; }
  else { const int lin = (bid & 7) * 32 + (bid >> 3); b = lin >> 7; h = (lin >> 3) & 15; s = lin & 7; }
  const int g = b * H + h, slot = g * 8 + s;
  const int rg = lane >> 3;

  if (wv == 0) {
    ST(0);
    __builtin_amdgcn_s_setprio(3);
    const int l56 = lane < 56 ? lane : 55;
    const int j16 = b * 8 + s, qc = j16 & 3, qk = j16 >> 2;
    const u32 epoch = __hip_atomic_load((gu32*)(G + G_E + h * 16), RLX_AGENT);
    const u32x4 xr0 = __builtin_nontemporal_load(reinterpret_cast<const u32x4*>(x + l56 * 8));
    const u32x4 xr1 = __builtin_nontemporal_load(reinterpret_cast<const u32x4*>(x + D + l56 * 8));
    u32x4 qw[14];
    const u32x4* p = reinterpret_cast<const u32x4*>(wq + (size_t)(qk * 112 + rg * 14) * (H * DH) + h * DH + qc * 64) + (lane & 7);
#pragma unroll
    for (int i = 0; i < 14; ++i) qw[i] = __builtin_nontemporal_load(p + i * (H * DH / 8));
    SB();
    {
      const u32x4 n0 = layernorm8(xr0, lane), n1 = layernorm8(xr1, lane);
      if (lane < 56) { *reinterpret_cast<u32x4*>(lds + L_XN + lane * 4) = n0; *reinterpret_cast<u32x4*>(lds + L_XN + 224 + lane * 4) = n1; }
    }
    lds_wait();
    const u32 tag = __builtin_amdgcn_readfirstlane(epoch) + 0x9E3779B9u;
    {
      float pa[8], pb[8], r0, r1;
#pragma unroll
      for (int j = 0; j < 8; ++j) { pa[j] = 0.f; pb[j] = 0.f; }
      const u32* xp = reinterpret_cast<const u32*>(lds + L_XN) + qk * 56 + rg * 7;
      u32 xa[7], xb[7];
#pragma unroll
      for (int i = 0; i < 7; ++i) { xa[i] = xp[i]; xb[i] = xp[224 + i]; }
      gemv_pairs<7, 0, 7>(qw, xa, xb, pa, pb);
      reduce_rg(pa, pb, lane, r0, r1);
      gu64* qo = G + G_Q + ((h * 2 + half) * 4 + qk) * 256 + qc * 64 + l32;
      put(qo, tag, __float_as_uint(r0));
      put(qo + 32, tag, __float_as_uint(r1));
    }
    __builtin_amdgcn_s_sleep(QDELAY);
    ST(1);
    {
      gu64* qg = G + G_Q + (h * 2 + b) * 1024 + half * 128 + l32;
      float qs[4];
#pragma unroll 1
      for (unsigned spin = 0; spin < SPIN_CAP; ++spin) {
        u64 v[16];
#pragma unroll
        for (int k = 0; k < 4; ++k)
#pragma unroll
          for (int j = 0; j < 4; ++j) v[k * 4 + j] = get(qg + k * 256 + j * 32);
        bool ok = true;
#pragma unroll
        for (int k = 0; k < 16; ++k) ok &= (u32)(v[k] >> 32) == tag;
#pragma unroll
        for (int j = 0; j < 4; ++j) qs[j] = (__uint_as_float((u32)v[j]) + __uint_as_float((u32)v[4 + j])) + (__uint_as_float((u32)v[8 + j]) + __uint_as_float((u32)v[12 + j]));
        if (__all(ok)) break;
      }
      const int pr = (l32 & 7) * 4 + ((l32 >> 4) & 1) * 2 + ((l32 >> 3) & 1);
      reinterpret_cast<u32*>(lds + L_Q)[(half * 2) * 32 + pr] = cvtpk(qs[0], qs[1]);
      reinterpret_cast<u32*>(lds + L_Q)[(half * 2 + 1) * 32 + pr] = cvtpk(qs[2], qs[3]);
    }
    ST(2);
    wg_barrier();
    __builtin_amdgcn_s_setprio(0);
    if (j16 == 0 && lane == 0) __hip_atomic_store((gu32*)(G + G_E + h * 16), tag, RLX_AGENT);
    if (g < 28) {
      const int rr = rg < 7 ? rg : 6;
      const u32* xp = reinterpret_cast<const u32*>(lds + L_XN) + s * 28 + rr * 4;
      u32 xa[4], xb[4];
#pragma unroll
      for (int i = 0; i < 4; ++i) { xa[i] = rg < 7 ? xp[i] : 0u; xb[i] = rg < 7 ? xp[224 + i] : 0u; }
#pragma unroll 1
      for (int mat = 0; mat < 2; ++mat) {
        const u32x4* pw = reinterpret_cast<const u32x4*>((mat ? wu : wg) + (size_t)(s * 56 + rr * 8) * F + g * 64) + (lane & 7);
        float pa[8], pb[8], r0, r1;
#pragma unroll
        for (int j = 0; j < 8; ++j) { pa[j] = 0.f; pb[j] = 0.f; }
        static_for<0, 2>([&](auto hc) {
          constexpr int hh = decltype(hc)::value;
          u32x4 t[4];
#pragma unroll
          for (int i = 0; i < 4; ++i) t[i] = __builtin_nontemporal_load(pw + (hh * 4 + i) * (F / 8));
          gemv_pairs<2, 2 * hh, 4>(t, xa, xb, pa, pb);
          SB();
        });
        reduce_rg(pa, pb, lane, r0, r1);
        const int col = (lane & 7) * 8 + ((lane >> 4) & 1) * 4 + ((lane >> 3) & 1) * 2;
        *reinterpret_cast<f32x2*>(P + P_F + ((s * 2 + mat) * 2 + half) * F + g * 64 + col) = f32x2{r0, r1};
      }
    }
    return;
  }

  const int w = wv - 1, tid = threadIdx.x - 64;
  const GAS char* const kbase = (const GAS char*)Kc + (size_t)slot * (1024 * 512) + w * 1024;
  const GAS char* const vbase = (const GAS char*)Vc + (size_t)slot * (1024 * 512) + w * 1024;
  const u32 loff = lane * 16;
  u32x4 buf[RING];
  const GAS char* lp = kbase;
  auto issue = [&](auto jc) {
    constexpr int j = decltype(jc)::value;
    asm volatile("s_waitcnt vmcnt(%0)" :: "i"(INFL - 1) : "memory");
    asm volatile("" : "+s"(lp));
    buf[j] = __builtin_nontemporal_load((const GAS u32x4*)(lp + loff));
    lp += NW * 1024;
  };
  static_for<0, RING>(issue);
  SB();
  ST(3);
  wg_barrier();
  const u32x4 qp = *reinterpret_cast<const u32x4*>(lds + L_Q + l32 * 4);
  ST(4);

  float* const pw = lds + L_P + (w * 2 + half) * NLD;
  float sr = 0.f, mx = -INFINITY;
#pragma unroll 1
  for (int it = 0; it < 4; ++it) {
    if (it == 3) lp = vbase;
    const int lj = l32 - ((it * RING) & 31);
    static_for<0, RING>([&](auto jc) {
      constexpr int j = decltype(jc)::value;
      const u32x4 kv = buf[j];
      float sacc = dot2(kv[0], qp[0], 0.f);
      sacc = dot2(kv[1], qp[1], sacc); sacc = dot2(kv[2], qp[2], sacc); sacc = dot2(kv[3], qp[3], sacc);
      asm volatile("" : "+v"(sacc));
      issue(jc);
      sacc = sum32(sacc);
      sr = (lj == j) ? sacc : sr;
      SB();
    });
    if ((((it + 1) * RING) & 31) == 0) {
      const float sv = rbf(sr) * C2;
      mx = fmaxf(mx, sv);
      pw[(it + 1) * RING - 32 + l32] = sv;
    }
    SB();
  }
  ST(5);
  float lsum = 0.f;
  mx = max64(mx);
#pragma unroll
  for (int k = 0; k < NLD / 32; ++k) {
    const float p = __builtin_amdgcn_exp2f(pw[k * 32 + l32] - mx);
    pw[k * 32 + l32] = p; lsum += p;
  }
  lsum = sum64(lsum);
  lds_wait();
  SB();

  f32x2 ac[4];
#pragma unroll
  for (int j = 0; j < 4; ++j) ac[j] = f32x2{0.f, 0.f};
  auto vbody = [&](auto refill, const float* pp) {
    f32x4 pr;
    static_for<0, RING>([&](auto jc) {
      constexpr int j = decltype(jc)::value;
      if constexpr ((j & 3) == 0) pr = *reinterpret_cast<const f32x4*>(pp + j);
      const u32x4 vv = buf[j];
      const float p = pr[j & 3];
#pragma unroll
      for (int k = 0; k < 4; ++k) ac[k] = __builtin_elementwise_fma(f32x2{p, p}, f32x2{bflo(vv[k]), bfhi(vv[k])}, ac[k]);
      asm volatile("" : "+v"(ac[0]), "+v"(ac[1]), "+v"(ac[2]), "+v"(ac[3]));
      if constexpr (decltype(refill)::value) issue(jc);
      SB();
    });
  };
#pragma unroll 1
  for (int it = 0; it < 3; ++it) vbody(IC<1>{}, pw + it * RING);
  if (lane == 0) { lds[L_ML + 2 * w] = mx; lds[L_ML + 2 * w + 1] = lsum; }
  vbody(IC<0>{}, pw + 3 * RING);
  ST(9);

  {
    float a[8];
#pragma unroll
    for (int j = 0; j < 4; ++j) { a[2 * j] = x32sum(ac[j][0]); a[2 * j + 1] = x32sum(ac[j][1]); }
    *reinterpret_cast<f32x4*>(lds + L_O + w * 256 + l32 * 8 + half * 4) = half ? f32x4{a[4], a[5], a[6], a[7]} : f32x4{a[0], a[1], a[2], a[3]};
    wg_barrier();
    if (tid < 256) {
      float M = -INFINITY, L = 0.f, o = 0.f;
#pragma unroll
      for (int ww = 0; ww < NW; ++ww) M = fmaxf(M, lds[L_ML + 2 * ww]);
#pragma unroll
      for (int ww = 0; ww < NW; ++ww) {
        const float sc = __builtin_amdgcn_exp2f(lds[L_ML + 2 * ww] - M);
        L = fmaf(sc, lds[L_ML + 2 * ww + 1], L);
        o = fmaf(sc, lds[L_O + ww * 256 + tid], o);
      }
      P[P_A + slot * 256 + tid] = o;
      if (tid == 0) *reinterpret_cast<f32x2*>(P + P_ML + slot * 2) = f32x2{M, L};
    }
  }
  ST(10);
}

__device__ __forceinline__ float sum8(float x) { x += dpp<XOR1>(x); x += dpp<XOR2>(x); x += dpp<HMIR>(x); return x; }
__device__ __forceinline__ float max8(float x) { x = fmaxf(x, dpp<XOR1>(x)); x = fmaxf(x, dpp<XOR2>(x)); x = fmaxf(x, dpp<HMIR>(x)); return x; }

__global__ void __launch_bounds__(256) tail_k(const u16* __restrict__ x, const u16* __restrict__ wo, const u16* __restrict__ wd, u64* ws, u16* __restrict__ out) {
  __shared__ __attribute__((aligned(16))) float lds[64 + 4 * 896 + 16];
  float* const lo = lds;
  float* const ly = lds + 64;
  float* const lr = ly + 4 * 896;
  gu64* const G = (gu64*)ws + (size_t)GOFF * 131072;
  const float* const P = reinterpret_cast<const float*>(ws + (size_t)GOFF * 131072 + G_P);
  const int tid = threadIdx.x, lane = tid & 63, w = __builtin_amdgcn_readfirstlane(tid >> 6), j = blockIdx.x;
  const int h = j >> 4, j16 = j & 15, l56 = lane < 56 ? lane : 55;
  const u32 epoch = __hip_atomic_load((gu32*)(G + G_E2), RLX_AGENT);
  const int pb = tid >> 7, pd = (tid >> 3) & 15, ps = tid & 7, slot = (pb * H + h) * 8 + ps;
  const float pa = P[P_A + slot * 256 + j16 * 16 + pd];
  const f32x2 ml = *reinterpret_cast<const f32x2*>(P + P_ML + slot * 2);
  const int fb = tid >> 7, fm = (tid >> 6) & 1, fu = (tid >> 3) & 7, fk = tid & 7;
  const float pf = P[P_F + ((fk * 2 + fm) * 2 + fb) * F + j * 7 + (fu < 7 ? fu : 6)];
  u32x4 wr[4], dr[2];
#pragma unroll
  for (int i = 0; i < 4; ++i) wr[i] = __builtin_nontemporal_load(reinterpret_cast<const u32x4*>(wo + (size_t)(j * 16 + 4 * w + i) * D) + l56);
#pragma unroll
  for (int i = 0; i < 2; ++i) { const int u = 2 * w + i; dr[i] = __builtin_nontemporal_load(reinterpret_cast<const u32x4*>(wd + (size_t)(j * 7 + (u < 7 ? u : 6)) * D) + l56); }
  u16 xv = 0;
  if (j < 224 && tid < 4) xv = x[(j / 112) * D + (j % 112) * 4 + tid];
  SB();
  {
    const float M = max8(ml[0]);
    const float e = __builtin_amdgcn_exp2f(ml[0] - M);
    const float L = sum8(e * ml[1]), o = sum8(e * pa);
    if (ps == 0) lo[pb * 16 + pd] = rbf(o / L);
    const float f = rbf(sum8(pf));
    if (fk == 0) ly[(fb * 2 + fm) * 8 + fu] = f;
  }
  wg_barrier();
  float hv[2][2];
#pragma unroll
  for (int bb = 0; bb < 2; ++bb)
#pragma unroll
    for (int i = 0; i < 2; ++i) {
      const int u = 2 * w + i;
      const float gv = ly[(bb * 2) * 8 + u], uv = ly[(bb * 2 + 1) * 8 + u];
      hv[bb][i] = u < 7 ? rbf(rbf(gv / (1.f + __expf(-gv))) * uv) : 0.f;
    }
  float y[2][8];
#pragma unroll
  for (int bb = 0; bb < 2; ++bb) {
    const f32x4 ov = *reinterpret_cast<const f32x4*>(lo + bb * 16 + 4 * w);
#pragma unroll
    for (int k = 0; k < 4; ++k) {
      float a = hv[bb][0] * bflo(dr[0][k]), c = hv[bb][0] * bfhi(dr[0][k]);
      a = fmaf(hv[bb][1], bflo(dr[1][k]), a); c = fmaf(hv[bb][1], bfhi(dr[1][k]), c);
#pragma unroll
      for (int i = 0; i < 4; ++i) { a = fmaf(ov[i], bflo(wr[i][k]), a); c = fmaf(ov[i], bfhi(wr[i][k]), c); }
      y[bb][2 * k] = a; y[bb][2 * k + 1] = c;
    }
  }
  wg_barrier();
  if (lane < 56) {
#pragma unroll
    for (int bb = 0; bb < 2; ++bb) {
      *reinterpret_cast<f32x4*>(ly + w * 896 + bb * D + lane * 8) = f32x4{y[bb][0], y[bb][1], y[bb][2], y[bb][3]};
      *reinterpret_cast<f32x4*>(ly + w * 896 + bb * D + lane * 8 + 4) = f32x4{y[bb][4], y[bb][5], y[bb][6], y[bb][7]};
    }
  }
  wg_barrier();
  const u32 tag = __builtin_amdgcn_readfirstlane(epoch) + 0x9E3779B9u;
#pragma unroll
  for (int r = 0; r < 4; ++r) {
    const int c = r * 256 + tid;
    if (c < 896) {
      const float t = (ly[c] + ly[896 + c]) + (ly[2 * 896 + c] + ly[3 * 896 + c]);
      put(G + G_Y + ((c >> 2) * 256 + j) * 4 + (c & 3), tag, __float_as_uint(t));
    }
  }
  if (j >= 224) return;
  {
    gu64* src = G + G_Y + (j * 256 + tid) * 4;
    float v[4] = {0.f, 0.f, 0.f, 0.f};
    bool ok = false;
#pragma unroll 1
    for (unsigned spin = 0; spin < SPIN_CAP; ++spin) {
      if (!ok) {
        ok = true;
#pragma unroll
        for (int k = 0; k < 4; ++k) { const u64 t = get(src + k); v[k] = __uint_as_float((u32)t); ok &= (u32)(t >> 32) == tag; }
      }
      if (__all(ok)) break;
    }
#pragma unroll
    for (int k = 0; k < 4; ++k) v[k] = sum64(v[k]);
    if (lane == 0) *reinterpret_cast<f32x4*>(lr + w * 4) = f32x4{v[0], v[1], v[2], v[3]};
  }
  wg_barrier();
  if (tid < 4) {
    const float r = __uint_as_float((u32)xv << 16) + ((lr[tid] + lr[4 + tid]) + (lr[8 + tid] + lr[12 + tid]));
    out[(j / 112) * D + (j % 112) * 4 + tid] = (u16)(cvtpk(r, r) & 0xffffu);
  }
  if (j == 0 && tid == 0) __hip_atomic_store((gu32*)(G + G_E2), tag, RLX_AGENT);
}

extern "C" void kernel_launch(void* const* d_in, const int* in_sizes, int n_in,
                              void* d_out, int out_size, void* d_ws, size_t ws_size,
                              hipStream_t stream) {
  main_k<<<NWG, NT, 0, stream>>>((const u16*)d_in[0], (const u32x4*)d_in[1], (const u32x4*)d_in[2], (const u16*)d_in[3],
                                 (const u16*)d_in[6], (const u16*)d_in[7], (u64*)d_ws);
  tail_k<<<NWG, 256, 0, stream>>>((const u16*)d_in[0], (const u16*)d_in[4], (const u16*)d_in[8], (u64*)d_ws, (u16*)d_out);
}
```

```cpp
#include <hip/hip_runtime.h>
#include <hip/hip_bf16.h>
#include <stdint.h>

#define MAPX 0
#define INFL 4
#define QDELAY 16
#define GOFF 0
#define EXTRA 16
#define FB 1
#define FB_OFF 300
#define FB_TOT 4000
#define FB_BAND 50
#define DL 2
#define DH_ 6
#define YMODE 2
#define CPF 0
#define PRET 1
#define ROT 0

typedef unsigned int u32;
typedef unsigned short u16;
typedef unsigned long long u64;
typedef __attribute__((ext_vector_type(4))) unsigned u32x4;
typedef __attribute__((ext_vector_type(4))) float f32x4;
typedef __attribute__((ext_vector_type(2))) float f32x2;
typedef __attribute__((ext_vector_type(2))) __bf16 bf16x2;
#define GAS __attribute__((address_space(1)))
#define LAS __attribute__((address_space(3)))
typedef GAS u32 gu32;
typedef GAS u64 gu64;
#define RLX_AGENT __ATOMIC_RELAXED, __HIP_MEMORY_SCOPE_AGENT

constexpr int D = 448, H = 16, SK = 8192, DH = 256, F = 1792;
constexpr int NWG = 256, NW = 8, NT = (NW + 2) * 64, RING = 8, WIN = 32, NLD = 64;
constexpr unsigned SPIN_CAP = 1u << 18;
constexpr float C2 = 0.0625f * 1.4426950408889634f;

constexpr int G_E = 0;
constexpr int G_T = 32;
constexpr int G_Q = G_T + 256;
constexpr int G_F = G_Q + 32 * 1024;
constexpr int G_ST = G_F + 28 * 2 * 8 * 128;
constexpr int G_A = G_ST + 32 * 16;
constexpr int G_Y = G_A + 256 * 256;

__device__ __forceinline__ float bflo(u32 v) { return __uint_as_float(v << 16); }
__device__ __forceinline__ float bfhi(u32 v) { return __uint_as_float(v & 0xffff0000u); }
__device__ __forceinline__ u32 cvtpk(float lo, float hi) {
  u32 r; asm("v_cvt_pk_bf16_f32 %0, %1, %2" : "=v"(r) : "v"(lo), "v"(hi)); return r;
}
__device__ __forceinline__ float rbf(float f) { return bflo(cvtpk(f, f)); }
__device__ __forceinline__ float dot2(u32 a, u32 b, float c) {
  return __builtin_amdgcn_fdot2_f32_bf16(__builtin_bit_cast(bf16x2, a), __builtin_bit_cast(bf16x2, b), c, false);
}
template <int CTRL> __device__ __forceinline__ float dpp(float x) {
  return __builtin_bit_cast(float, __builtin_amdgcn_mov_dpp(__builtin_bit_cast(int, x), CTRL, 0xf, 0xf, true));
}
constexpr int XOR1 = 0xB1, XOR2 = 0x4E, HMIR = 0x141, MIR = 0x140, ROR8 = 0x128;
__device__ __forceinline__ float sum16(float x) {
  x += dpp<XOR1>(x); x += dpp<XOR2>(x); x += dpp<HMIR>(x); x += dpp<MIR>(x); return x;
}
__device__ __forceinline__ float max16(float x) {
  x = fmaxf(x, dpp<XOR1>(x)); x = fmaxf(x, dpp<XOR2>(x)); x = fmaxf(x, dpp<HMIR>(x)); x = fmaxf(x, dpp<MIR>(x)); return x;
}
__device__ __forceinline__ float x16sum(float x) {
  auto s = __builtin_amdgcn_permlane16_swap(__float_as_uint(x), __float_as_uint(x), false, false);
  return __uint_as_float(s[0]) + __uint_as_float(s[1]);
}
__device__ __forceinline__ float x32sum(float x) {
  auto s = __builtin_amdgcn_permlane32_swap(__float_as_uint(x), __float_as_uint(x), false, false);
  return __uint_as_float(s[0]) + __uint_as_float(s[1]);
}
__device__ __forceinline__ float x16max(float x) {
  auto s = __builtin_amdgcn_permlane16_swap(__float_as_uint(x), __float_as_uint(x), false, false);
  return fmaxf(__uint_as_float(s[0]), __uint_as_float(s[1]));
}
__device__ __forceinline__ float x32max(float x) {
  auto s = __builtin_amdgcn_permlane32_swap(__float_as_uint(x), __float_as_uint(x), false, false);
  return fmaxf(__uint_as_float(s[0]), __uint_as_float(s[1]));
}
__device__ __forceinline__ float sum32(float x) { return x16sum(sum16(x)); }
__device__ __forceinline__ float sum64(float x) { return x32sum(x16sum(sum16(x))); }
__device__ __forceinline__ float max64(float x) { return x32max(x16max(max16(x))); }
#define SB() __builtin_amdgcn_sched_barrier(0)
template <int I> struct IC { static constexpr int value = I; };
template <int B, int E, typename Fn> __device__ __forceinline__ void static_for(Fn&& f) {
  if constexpr (B < E) { f(IC<B>{}); static_for<B + 1, E>(f); }
}
__device__ __forceinline__ void lds_wait() { asm volatile("s_waitcnt lgkmcnt(0)" ::: "memory"); }
__device__ __forceinline__ void wg_barrier() { lds_wait(); __builtin_amdgcn_s_barrier(); asm volatile("" ::: "memory"); }
__device__ __forceinline__ void put(gu64* g, u32 tag, u32 v) { __hip_atomic_store(g, ((u64)tag << 32) | v, RLX_AGENT); }
__device__ __forceinline__ u64 get(gu64* g) { return __hip_atomic_load(g, RLX_AGENT); }

__device__ __forceinline__ float treduce32(float (&pv)[32], int lane) {
#pragma unroll
  for (int j = 0; j < 16; ++j) {
    auto t = __builtin_amdgcn_permlane32_swap(__float_as_uint(pv[j]), __float_as_uint(pv[j + 16]), false, false);
    pv[j] = __uint_as_float(t[0]) + __uint_as_float(t[1]);
  }
#pragma unroll
  for (int j = 0; j < 8; ++j) {
    auto t = __builtin_amdgcn_permlane16_swap(__float_as_uint(pv[j]), __float_as_uint(pv[j + 8]), false, false);
    pv[j] = __uint_as_float(t[0]) + __uint_as_float(t[1]);
  }
  float mine = 0.f;
#pragma unroll
  for (int j = 0; j < 8; ++j) { const float t = sum16(pv[j]); mine = ((lane & 15) == j) ? t : mine; }
  return mine;
}

#define ST(k)
#define STT(k)

constexpr int L_Q = 0;
constexpr int L_TAG = L_Q + 128;
constexpr int L_XN = L_TAG + 4;
constexpr int L_P = L_XN + 448;
constexpr int L_O = L_P + 1024;
constexpr int L_ML = L_O + NW * 256;
constexpr int L_ON = L_ML + 16;
constexpr int L_YP = L_ON + 32;
constexpr int L_H = L_YP + NW * D;
constexpr int L_RED = L_H + 16;
constexpr int L_END = L_RED + 8;
__device__ __forceinline__ void lds_spin(float* lds, int idx, u32 want) {
  volatile LAS u32* p = (volatile LAS u32*)(lds + idx);
#pragma unroll 1
  for (unsigned spin = 0; spin < (SPIN_CAP << 4); ++spin) {
    if ((u32)__builtin_amdgcn_readfirstlane(*p) >= want) break;
    __builtin_amdgcn_s_sleep(2);
  }
  asm volatile("" ::: "memory");
}
__device__ __forceinline__ float sum8(float x) { x += dpp<XOR1>(x); x += dpp<XOR2>(x); x += dpp<HMIR>(x); return x; }
__device__ __forceinline__ float max8(float x) { x = fmaxf(x, dpp<XOR1>(x)); x = fmaxf(x, dpp<XOR2>(x)); x = fmaxf(x, dpp<HMIR>(x)); return x; }

__device__ __forceinline__ u32x4 layernorm8(const u32x4 xr, int lane) {
  float v[8];
#pragma unroll
  for (int j = 0; j < 4; ++j) { v[2 * j] = bflo(xr[j]); v[2 * j + 1] = bfhi(xr[j]); }
  float sm = ((v[0] + v[1]) + (v[2] + v[3])) + ((v[4] + v[5]) + (v[6] + v[7]));
  sm = lane < 56 ? sm : 0.f;
  const float mean = sum64(sm) * (1.0f / D);
  float sq = 0.f;
#pragma unroll
  for (int j = 0; j < 8; ++j) { const float d = v[j] - mean; sq += d * d; }
  sq = lane < 56 ? sq : 0.f;
  const float var = rbf(sum64(sq) * (1.0f / D));
  const float rden = 1.0f / rbf(sqrtf(rbf(var + 1e-5f)));
  const float mb = rbf(mean);
  u32x4 o;
#pragma unroll
  for (int j = 0; j < 4; ++j) {
    const u32 d = cvtpk(v[2 * j] - mb, v[2 * j + 1] - mb);
    o[j] = cvtpk(bflo(d) * rden, bfhi(d) * rden);
  }
  return o;
}
template <int NP, int OFF, int N>
__device__ __forceinline__ void gemv_pairs(const u32x4 (&t)[2 * NP], const u32 (&xa)[N], const u32 (&xb)[N], float (&pa)[8], float (&pb)[8]) {
#pragma unroll
  for (int i = 0; i < NP; ++i) {
    const u32 x0 = xa[OFF + i], x1 = xb[OFF + i];
#pragma unroll
    for (int j = 0; j < 4; ++j) {
      const u32 lo = __builtin_amdgcn_perm(t[2 * i + 1][j], t[2 * i][j], 0x05040100u), hi = __builtin_amdgcn_perm(t[2 * i + 1][j], t[2 * i][j], 0x07060302u);
      pa[2 * j] = dot2(lo, x0, pa[2 * j]); pa[2 * j + 1] = dot2(hi, x0, pa[2 * j + 1]);
      pb[2 * j] = dot2(lo, x1, pb[2 * j]); pb[2 * j + 1] = dot2(hi, x1, pb[2 * j + 1]);
    }
  }
}
__device__ __forceinline__ void reduce_rg(float (&pa)[8], float (&pb)[8], int lane, float& r0, float& r1) {
#pragma unroll
  for (int j = 0; j < 8; ++j) {
    auto t = __builtin_amdgcn_permlane32_swap(__float_as_uint(pa[j]), __float_as_uint(pb[j]), false, false);
    pa[j] = __uint_as_float(t[0]) + __uint_as_float(t[1]);
  }
#pragma unroll
  for (int j = 0; j < 4; ++j) {
    auto t = __builtin_amdgcn_permlane16_swap(__float_as_uint(pa[j]), __float_as_uint(pa[j + 4]), false, false);
    pa[j] = __uint_as_float(t[0]) + __uint_as_float(t[1]);
  }
  float a0 = pa[0] + dpp<ROR8>(pa[0]), a1 = pa[1] + dpp<ROR8>(pa[1]), a2 = pa[2] + dpp<ROR8>(pa[2]), a3 = pa[3] + dpp<ROR8>(pa[3]);
  asm volatile("" : "+v"(a0), "+v"(a1), "+v"(a2), "+v"(a3));
  const bool bit3 = (lane >> 3) & 1;
  r0 = bit3 ? a2 : a0; r1 = bit3 ? a3 : a1;
}

__global__ void __launch_bounds__(NT) main_k(const u16* __restrict__ x, const u32x4* __restrict__ Kc, const u32x4* __restrict__ Vc,
                                            const u16* __restrict__ wq, const u16* __restrict__ wo, const u16* __restrict__ wg,
                                            const u16* __restrict__ wu, const u16* __restrict__ wd, u64* ws, u16* __restrict__ out) {
  __shared__ __attribute__((aligned(16))) float lds[L_END];
  gu64* const G = (gu64*)ws + (size_t)GOFF * 131072;
  const int lane = threadIdx.x & 63, half = lane >> 5, l32 = lane & 31;
  const int wv = __builtin_amdgcn_readfirstlane(threadIdx.x >> 6);
  const int bid = blockIdx.x;
  const int slot = MAPX == 0 ? bid : (bid & 7) * 32 + (bid >> 3);
  const int g = slot >> 3, s = slot & 7, b = g >> 4, h = g & 15;
  const int rg = lane >> 3, l56 = lane < 56 ? lane : 55;

  if (wv < 2) {
    ST(0);
    if (wv == 1 && g >= 28) {
      __builtin_amdgcn_s_sleep(127);
      if (PRET) {
        const int ib = h * 8 + s;
        const GAS char* pt = lane == 0 ? (const GAS char*)wo + (size_t)(h * DH + s * 32) * (D * 2) : lane == 1 ? (const GAS char*)wd + (size_t)((ib < 112 ? ib : 0) * 16) * (D * 2)
                           : lane == 2 ? (const GAS char*)out : lane == 3 ? (const GAS char*)(G + G_A + slot * 256) : lane == 4 ? (const GAS char*)(G + G_Y + slot * D)
                           : (const GAS char*)Vc + (size_t)slot * (1024 * 512);
        u32 d0 = __builtin_nontemporal_load((const GAS u32*)pt);
        asm volatile("" :: "v"(d0));
      }
      return;
    }
    const u32 tstart = (u32)__builtin_amdgcn_s_memrealtime();
    __builtin_amdgcn_s_setprio(3);
    const int j16 = b * 8 + s, qc = j16 & 3, qk = j16 >> 2;
    const u32 epoch = __hip_atomic_load((gu32*)(G + G_E), RLX_AGENT);
    const u32x4 xr0 = __builtin_nontemporal_load(reinterpret_cast<const u32x4*>(x + l56 * 8));
    const u32x4 xr1 = __builtin_nontemporal_load(reinterpret_cast<const u32x4*>(x + D + l56 * 8));
    const int rr = rg < 7 ? rg : 6;
    const int row0 = (wv ? s * 56 : qk * 112) + rr * 8, row1 = wv ? row0 : row0 + 56;
    const int ld8 = wv ? F / 8 : H * DH / 8, col8 = (wv ? g * 8 : h * 32 + qc * 8) + (lane & 7);
    const u32x4* p0 = reinterpret_cast<const u32x4*>(wv ? wg : wq) + (size_t)row0 * ld8 + col8;
    const u32x4* p1 = reinterpret_cast<const u32x4*>(wv ? wu : wq) + (size_t)row1 * ld8 + col8;
    u32x4 t0[8], t1[8];
#pragma unroll
    for (int i = 0; i < 8; ++i) t0[i] = __builtin_nontemporal_load(p0 + i * ld8);
#pragma unroll
    for (int i = 0; i < 8; ++i) t1[i] = __builtin_nontemporal_load(p1 + i * ld8);
    SB();
    const u32x4 n0 = layernorm8(xr0, lane), n1 = layernorm8(xr1, lane);
    const u32 tag = __builtin_amdgcn_readfirstlane(epoch) + 0x9E3779B9u;
    float pa[8], pb[8], r0, r1;
#pragma unroll
    for (int j = 0; j < 8; ++j) { pa[j] = 0.f; pb[j] = 0.f; }
    gu64* po = wv ? G + G_F + ((g * 2 + half) * 8 + s) * 128 + l32 : G + G_Q + ((h * 2 + half) * 4 + qk) * 256 + qc * 64 + l32;
#pragma unroll 1
    for (int blk = 0; blk < 2; ++blk) {
      const int src = ((blk ? row1 : row0) >> 3) * 4;
      u32 xa[4], xb[4];
#pragma unroll
      for (int i = 0; i < 4; ++i) {
        const u32 ta = __builtin_amdgcn_ds_bpermute(src, n0[i]), tb = __builtin_amdgcn_ds_bpermute(src, n1[i]);
        xa[i] = rg < 7 ? ta : 0u; xb[i] = rg < 7 ? tb : 0u;
      }
      gemv_pairs<4, 0, 4>(t0, xa, xb, pa, pb);
#pragma unroll
      for (int i = 0; i < 8; ++i) t0[i] = t1[i];
      if (wv || blk) {
        reduce_rg(pa, pb, lane, r0, r1);
        put(po, tag, __float_as_uint(r0)); put(po + 32, tag, __float_as_uint(r1));
        po += 64;
#pragma unroll
        for (int j = 0; j < 8; ++j) { pa[j] = 0.f; pb[j] = 0.f; }
      }
    }
    if (wv) {
      if (PRET) {
        const int ib = h * 8 + s;
        const GAS char* pt = lane == 0 ? (const GAS char*)wo + (size_t)(h * DH + s * 32) * (D * 2) : lane == 1 ? (const GAS char*)wd + (size_t)((ib < 112 ? ib : 0) * 16) * (D * 2)
                           : lane == 2 ? (const GAS char*)out : lane == 3 ? (const GAS char*)(G + G_A + slot * 256) : lane == 4 ? (const GAS char*)(G + G_Y + slot * D)
                           : (const GAS char*)Vc + (size_t)slot * (1024 * 512);
        u32 d0 = __builtin_nontemporal_load((const GAS u32*)pt);
        asm volatile("" :: "v"(d0));
      }
      return;
    }
    if (lane == 0) put(G + G_T + h * 16 + j16, tag, tstart);
    __builtin_amdgcn_s_sleep(QDELAY);
    ST(1);
    {
      gu64* qg = G + G_Q + (h * 2 + b) * 1024 + half * 128 + l32;
      float qs[4]; u32 tv = 0;
#pragma unroll 1
      for (unsigned spin = 0; spin < SPIN_CAP; ++spin) {
        const u64 tt = get(G + G_T + h * 16 + (lane & 15));
        tv = (u32)tt;
        if (!__all((u32)(tt >> 32) == tag)) { __builtin_amdgcn_s_sleep(4); continue; }
        u64 v[16];
#pragma unroll
        for (int k = 0; k < 4; ++k)
#pragma unroll
          for (int j = 0; j < 4; ++j) v[k * 4 + j] = get(qg + k * 256 + j * 32);
        bool ok = true;
#pragma unroll
        for (int k = 0; k < 16; ++k) ok &= (u32)(v[k] >> 32) == tag;
#pragma unroll
        for (int j = 0; j < 4; ++j) qs[j] = (__uint_as_float((u32)v[j]) + __uint_as_float((u32)v[4 + j])) + (__uint_as_float((u32)v[8 + j]) + __uint_as_float((u32)v[12 + j]));
        if (__all(ok)) break;
      }
      const int pr = (l32 & 7) * 4 + ((l32 >> 4) & 1) * 2 + ((l32 >> 3) & 1);
      reinterpret_cast<u32*>(lds + L_Q)[(half * 2) * 32 + pr] = cvtpk(qs[0], qs[1]);
      reinterpret_cast<u32*>(lds + L_Q)[(half * 2 + 1) * 32 + pr] = cvtpk(qs[2], qs[3]);
      const float early = -max16((float)(int)(tstart - tv));
      if (lane == 0) { reinterpret_cast<u32*>(lds)[L_TAG] = tag; reinterpret_cast<u32*>(lds)[L_TAG + 1] = tstart + (u32)(int)early; }
    }
    ST(2);
    wg_barrier();
    return;
  }

  const int w = wv - 2, tid = threadIdx.x - 128;
  u32x4 buf[WIN];
  const GAS char* lp = (const GAS char*)Kc + (size_t)slot * (1024 * 512) + w * 1024;
  u32 loff = lane * 16;
  long long st_n = 8192, st_3 = 8192, st_5 = 8192;
  int lag = 0; u32 tref = 0;
  auto pace = [&](int n) {
    if (FB) {
      lag = __builtin_amdgcn_readfirstlane((int)((u32)__builtin_amdgcn_s_memrealtime() - tref) - (FB_OFF + n * FB_TOT / 128));
    }
  };
  auto issue = [&](auto jc) {
    constexpr int j = decltype(jc)::value;
    asm volatile("s_waitcnt vmcnt(%1)\n\ts_cmpk_gt_i32 %0, %4\n\ts_cbranch_scc1 1f\n\ts_waitcnt vmcnt(%2)\n\ts_cmpk_lt_i32 %0, %5\n\ts_cbranch_scc0 1f\n\ts_waitcnt vmcnt(%3)\n1:"
                 :: "s"(lag), "i"(DH_ - 1), "i"(INFL - 1), "i"(DL - 1), "i"(FB_BAND), "i"(-FB_BAND) : "memory", "scc");
    asm volatile("" : "+s"(lp));
    buf[j] = __builtin_nontemporal_load((const GAS u32x4*)(lp + loff));
    lp += j == 3 ? st_3 : (j >= 5 && j < RING) ? st_5 : st_n;
  };
  static_for<0, WIN>([&](auto jc) {
    constexpr int j = decltype(jc)::value;
    asm volatile("s_waitcnt vmcnt(%0)" :: "i"(INFL - 1) : "memory");
    asm volatile("" : "+s"(lp));
    buf[j] = __builtin_nontemporal_load((const GAS u32x4*)(lp + loff));
    lp += 8192;
  });
  SB();
  ST(3);
  wg_barrier();
  const u32x4 qp = *reinterpret_cast<const u32x4*>(lds + L_Q + l32 * 4);
  const u32 tag = __builtin_amdgcn_readfirstlane(reinterpret_cast<const u32*>(lds)[L_TAG]);
  tref = __builtin_amdgcn_readfirstlane(reinterpret_cast<const u32*>(lds)[L_TAG + 1]);
  ST(4);

  float* const pw = lds + L_P + (w * 2 + half) * NLD;
  float sr = 0.f, s0 = 0.f;
  pace(WIN);
  static_for<0, WIN>([&](auto jc) {
    constexpr int j = decltype(jc)::value;
    const u32x4 kv = buf[j];
    float sacc = dot2(kv[0], qp[0], 0.f);
    sacc = dot2(kv[1], qp[1], sacc); sacc = dot2(kv[2], qp[2], sacc); sacc = dot2(kv[3], qp[3], sacc);
    asm volatile("" : "+v"(sacc));
    if constexpr ((j & 3) == 0) issue(IC<j / 4>{});
    sacc = sum32(sacc);
    s0 = (l32 == j) ? sacc : s0;
    asm volatile("" : "+v"(s0));
    SB();
  });
#pragma unroll 1
  for (int it = 0; it < 4; ++it) {
    if (it == 3) lp = (const GAS char*)Vc + (size_t)slot * (1024 * 512) + w * 1024;
    pace(40 + it * 8);
    const int lj = l32 - it * 8;
    static_for<0, RING>([&](auto jc) {
      constexpr int j = decltype(jc)::value;
      const u32x4 kv = buf[j];
      float sacc = dot2(kv[0], qp[0], 0.f);
      sacc = dot2(kv[1], qp[1], sacc); sacc = dot2(kv[2], qp[2], sacc); sacc = dot2(kv[3], qp[3], sacc);
      asm volatile("" : "+v"(sacc));
      issue(jc);
      sacc = sum32(sacc);
      sr = (lj == j) ? sacc : sr;
      SB();
    });
  }
  ST(5);
  {
    const float a = rbf(s0) * C2, c = rbf(sr) * C2;
    const float mx = max64(fmaxf(a, c));
    const float p0 = __builtin_amdgcn_exp2f(a - mx), p1 = __builtin_amdgcn_exp2f(c - mx);
    pw[l32] = p0; pw[32 + l32] = p1;
    const float ls = sum64(p0 + p1);
    if (lane == 0) { lds[L_ML + 2 * w] = mx; lds[L_ML + 2 * w + 1] = ls; }
  }
  u16 xv = 0;
  constexpr int NCONS = YMODE == 2 ? 56 : 224, CCOL = 896 / NCONS;
  if (bid < NCONS && tid < CCOL) xv = x[bid * CCOL + tid];
  lds_wait();
  SB();

  f32x2 ac[4];
#pragma unroll
  for (int j = 0; j < 4; ++j) ac[j] = f32x2{0.f, 0.f};
#pragma unroll 1
  for (int it = 0; it < 8; ++it) {
    pace(72 + it * 8);
    if (CPF && it == CPF && w == 0) {
      u64 pc; asm volatile("s_getpc_b64 %0" : "=s"(pc));
      const GAS u32* cp = (const GAS u32*)(pc & ~127ull) + (lane < 36 ? lane : 35) * 32;
      u32 d0 = cp[0];
      asm volatile("" :: "v"(d0));
    }
    if (it == 7) {
      const int ib = h * 8 + s;
      lp = (const GAS char*)wo + (size_t)(h * DH + s * 32 + 4 * w) * (D * 2);
      const GAS char* wdp = (const GAS char*)wd + (size_t)((ib < 112 ? ib : 0) * 16 + 2 * w) * (D * 2);
      loff = l56 * 16; st_n = D * 2; st_3 = wdp - (lp + 3 * D * 2); st_5 = 0; lag = 10000;
    }
    f32x4 pr;
    static_for<0, RING>([&](auto jc) {
      constexpr int j = decltype(jc)::value;
      if constexpr ((j & 3) == 0) pr = *reinterpret_cast<const f32x4*>(pw + it * 8 + j);
      const u32x4 vv = buf[j];
      const float p = pr[j & 3];
#pragma unroll
      for (int k = 0; k < 4; ++k) ac[k] = __builtin_elementwise_fma(f32x2{p, p}, f32x2{bflo(vv[k]), bfhi(vv[k])}, ac[k]);
      asm volatile("" : "+v"(ac[0]), "+v"(ac[1]), "+v"(ac[2]), "+v"(ac[3]));
      issue(jc);
      SB();
    });
  }
  ST(9);

  {
    float a[8];
#pragma unroll
    for (int j = 0; j < 4; ++j) { a[2 * j] = x32sum(ac[j][0]); a[2 * j + 1] = x32sum(ac[j][1]); }
    *reinterpret_cast<f32x4*>(lds + L_O + w * 256 + l32 * 8 + half * 4) = half ? f32x4{a[4], a[5], a[6], a[7]} : f32x4{a[0], a[1], a[2], a[3]};
    wg_barrier();
    if (tid < 256) {
      float M = -INFINITY, L = 0.f, o = 0.f;
#pragma unroll
      for (int ww = 0; ww < NW; ++ww) M = fmaxf(M, lds[L_ML + 2 * ww]);
#pragma unroll
      for (int ww = 0; ww < NW; ++ww) {
        const float sc = __builtin_amdgcn_exp2f(lds[L_ML + 2 * ww] - M);
        L = fmaf(sc, lds[L_ML + 2 * ww + 1], L);
        o = fmaf(sc, lds[L_O + ww * 256 + tid], o);
      }
      put(G + G_A + slot * 256 + tid, tag, __float_as_uint(o));
      if (tid < 2) put(G + G_ST + slot * 2 + tid, tag, __float_as_uint(tid ? L : M));
      if (w == 3) {
        float hv = 0.f;
        const int ib = h * 8 + s;
        if (ib < 112) {
          const int col = (ib & 3) * 16 + (lane & 15);
          const int pos = (col & 1) * 32 + (col >> 3) + ((col >> 2) & 1) * 16 + ((col >> 1) & 1) * 8;
          gu64* fg = G + G_F + ((((ib >> 2) * 2 + b) * 8 + half * 4) * 2 + ((lane >> 4) & 1)) * 64 + pos;
          float sum = 0.f;
#pragma unroll 1
          for (unsigned spin = 0; spin < SPIN_CAP; ++spin) {
            bool ok = true; sum = 0.f;
#pragma unroll
            for (int k = 0; k < 4; ++k) { const u64 v = get(fg + k * 128); sum += __uint_as_float((u32)v); ok &= (u32)(v >> 32) == tag; }
            if (__all(ok)) break;
            __builtin_amdgcn_s_sleep(8);
          }
          sum = rbf(x32sum(sum));
          auto sw = __builtin_amdgcn_permlane16_swap(__float_as_uint(sum), __float_as_uint(sum), false, false);
          const float gv = __uint_as_float(sw[0]), uv = __uint_as_float(sw[1]);
          hv = rbf(rbf(gv / (1.f + __expf(-gv))) * uv);
        }
        if (lane < 16) lds[L_H + lane] = hv;
      }
    } else {
      const int t = tid - 256, d = t >> 3, sp = t & 7;
      gu64* pa = G + G_A + (g * 8 + sp) * 256 + s * 32 + d;
      gu64* pm = G + G_ST + (g * 8 + sp) * 2;
      float av = 0.f, mv = 0.f, lv = 0.f;
      bool ok = false;
#pragma unroll 1
      for (unsigned spin = 0; spin < SPIN_CAP; ++spin) {
        if (!ok) {
          const u64 ta = get(pa), tm = get(pm), tl = get(pm + 1);
          av = __uint_as_float((u32)ta); mv = __uint_as_float((u32)tm); lv = __uint_as_float((u32)tl);
          ok = (u32)(ta >> 32) == tag && (u32)(tm >> 32) == tag && (u32)(tl >> 32) == tag;
        }
        if (__all(ok)) break;
        __builtin_amdgcn_s_sleep(1);
      }
      const float M = max8(mv);
      const float e = __builtin_amdgcn_exp2f(mv - M);
      const float L = sum8(e * lv), o = sum8(e * av);
      if (sp == 0) lds[L_ON + d] = o / L;
    }
    float y[8];
    {
#pragma unroll
      for (int k = 0; k < 3; ++k) {
        const u32x4 a = buf[2 * k], c = buf[2 * k + 1];
#pragma unroll
        for (int j = 0; j < 4; ++j) { buf[2 * k][j] = __builtin_amdgcn_perm(c[j], a[j], 0x05040100u); buf[2 * k + 1][j] = __builtin_amdgcn_perm(c[j], a[j], 0x07060302u); }
      }
    }
    wg_barrier();
    ST(10);
    {
      const f32x2 hh = *reinterpret_cast<const f32x2*>(lds + L_H + 2 * w);
      const u32 h01 = cvtpk(hh[0], hh[1]);
#pragma unroll
      for (int j = 0; j < 4; ++j) { y[2 * j] = dot2(buf[4][j], h01, 0.f); y[2 * j + 1] = dot2(buf[5][j], h01, 0.f); }
    }
    const f32x4 ov = *reinterpret_cast<const f32x4*>(lds + L_ON + 4 * w);
    const u32 o01 = cvtpk(ov[0], ov[1]), o23 = cvtpk(ov[2], ov[3]);
#pragma unroll
    for (int j = 0; j < 4; ++j) {
      y[2 * j] = dot2(buf[2][j], o23, dot2(buf[0][j], o01, y[2 * j]));
      y[2 * j + 1] = dot2(buf[3][j], o23, dot2(buf[1][j], o01, y[2 * j + 1]));
    }
    if (lane < 56) {
      *reinterpret_cast<f32x4*>(lds + L_YP + w * D + lane * 8) = f32x4{y[0], y[1], y[2], y[3]};
      *reinterpret_cast<f32x4*>(lds + L_YP + w * D + lane * 8 + 4) = f32x4{y[4], y[5], y[6], y[7]};
    }
    wg_barrier();
    if (tid < D) {
      float t = 0.f;
#pragma unroll
      for (int ww = 0; ww < NW; ++ww) t += lds[L_YP + ww * D + tid];
      put(YMODE == 0 ? G + G_Y + ((b * 112 + (tid >> 2)) * 128 + (slot & 127)) * 4 + (tid & 3) : G + G_Y + slot * D + tid, tag, __float_as_uint(t));
    }
  }
  ST(8);

  if (bid < NCONS) {
    if (YMODE < 2) {
      if (w < 2) {
        gu64* src = YMODE == 0 ? G + G_Y + (bid * 128 + tid) * 4 : G + G_Y + ((bid / 112) * 128 + tid) * D + (bid % 112) * 4;
        float v[4] = {0.f, 0.f, 0.f, 0.f};
        bool ok = false;
#pragma unroll 1
        for (unsigned spin = 0; spin < SPIN_CAP; ++spin) {
          if (!ok) {
            ok = true;
#pragma unroll
            for (int k = 0; k < 4; ++k) { const u64 t = get(src + k); v[k] = __uint_as_float((u32)t); ok &= (u32)(t >> 32) == tag; }
          }
          if (__all(ok)) break;
          __builtin_amdgcn_s_sleep(2);
        }
#pragma unroll
        for (int k = 0; k < 4; ++k) v[k] = sum64(v[k]);
        if (lane == 0) *reinterpret_cast<f32x4*>(lds + L_O + w * 4) = f32x4{v[0], v[1], v[2], v[3]};
      }
      wg_barrier();
      if (tid < 4) {
        const float r = __uint_as_float((u32)xv << 16) + (lds[L_O + tid] + lds[L_O + 4 + tid]);
        out[bid * 4 + tid] = (u16)(cvtpk(r, r) & 0xffffu);
      }
    } else {
      gu64* src = G + G_Y + ((bid / 28) * 128 + (tid >> 4) * 4) * D + (bid % 28) * 16 + (tid & 15);
      float v = 0.f;
      bool ok = false;
#pragma unroll 1
      for (unsigned spin = 0; spin < SPIN_CAP; ++spin) {
        if (!ok) {
          ok = true; v = 0.f;
#pragma unroll
          for (int k = 0; k < 4; ++k) { const u64 t = get(src + k * D); v += __uint_as_float((u32)t); ok &= (u32)(t >> 32) == tag; }
        }
        if (__all(ok)) break;
        __builtin_amdgcn_s_sleep(1);
      }
      v = x32sum(x16sum(v));
      if (lane < 16) lds[L_O + w * 16 + lane] = v;
      wg_barrier();
      if (tid < 16) {
        float r = 0.f;
#pragma unroll
        for (int ww = 0; ww < NW; ++ww) r += lds[L_O + ww * 16 + tid];
        r += __uint_as_float((u32)xv << 16);
        out[bid * 16 + tid] = (u16)(cvtpk(r, r) & 0xffffu);
      }
    }
  } else if (bid == NWG - 1) {
    if (w < 4) {
      bool ok = false;
#pragma unroll 1
      for (unsigned spin = 0; spin < SPIN_CAP; ++spin) {
        if (!ok) ok = (u32)(get(YMODE == 0 ? G + G_Y + ((tid >> 7) * 112 * 128 + (tid & 127)) * 4 : G + G_Y + tid * D) >> 32) == tag;
        if (__all(ok)) break;
        __builtin_amdgcn_s_sleep(2);
      }
    }
    wg_barrier();
    if (tid == 0) __hip_atomic_store((gu32*)(G + G_E), tag, RLX_AGENT);
  }
  ST(11);
}

extern "C" void kernel_launch(void* const* d_in, const int* in_sizes, int n_in,
                              void* d_out, int out_size, void* d_ws, size_t ws_size,
                              hipStream_t stream) {
  main_k<<<NWG, NT, 0, stream>>>((const u16*)d_in[0], (const u32x4*)d_in[1], (const u32x4*)d_in[2], (const u16*)d_in[3],
                                 (const u16*)d_in[4], (const u16*)d_in[6], (const u16*)d_in[7], (const u16*)d_in[8],
                                 (u64*)d_ws, (u16*)d_out);
}
```

```cpp
#include <hip/hip_runtime.h>
#include <hip/hip_bf16.h>
#include <stdint.h>

#define MAPX 0
#define INFL 4
#define QDELAY 16
#define GOFF 0
#define EXTRA 16
#define FB 1
#define FB_OFF 150
#define FB_TOT 3600
#define FB_BAND 50
#define DL 2
#define DH_ 6
#define YMODE 2
#define CPF 0
#define PRET 0
#define KMODE 0
#define EARLY 0
#define PS1 1
#define PIPE 0
#define PGAP 5
#define FD1 20
#define FD2 12
#define ROT 0

typedef unsigned int u32;
typedef unsigned short u16;
typedef unsigned long long u64;
typedef __attribute__((ext_vector_type(4))) unsigned u32x4;
typedef __attribute__((ext_vector_type(4))) float f32x4;
typedef __attribute__((ext_vector_type(2))) float f32x2;
typedef __attribute__((ext_vector_type(2))) __bf16 bf16x2;
#define GAS __attribute__((address_space(1)))
#define LAS __attribute__((address_space(3)))
typedef GAS u32 gu32;
typedef GAS u64 gu64;
#define RLX_AGENT __ATOMIC_RELAXED, __HIP_MEMORY_SCOPE_AGENT

constexpr int D = 448, H = 16, SK = 8192, DH = 256, F = 1792;
constexpr int NWG = 256, NW = 8, NT = (NW + 2) * 64, RING = 8, WIN = 32, NLD = 64;
constexpr unsigned SPIN_CAP = 1u << 18;
constexpr float C2 = 0.0625f * 1.4426950408889634f;

constexpr int G_E = 0;
constexpr int G_T = 32;
constexpr int G_Q = G_T + 256;
constexpr int G_F = G_Q + 32 * 1024;
constexpr int G_ST = G_F + 28 * 2 * 8 * 128;
constexpr int G_A = G_ST + 32 * 16;
constexpr int G_Y = G_A + 256 * 256;

__device__ __forceinline__ float bflo(u32 v) { return __uint_as_float(v << 16); }
__device__ __forceinline__ float bfhi(u32 v) { return __uint_as_float(v & 0xffff0000u); }
__device__ __forceinline__ u32 cvtpk(float lo, float hi) {
  u32 r; asm("v_cvt_pk_bf16_f32 %0, %1, %2" : "=v"(r) : "v"(lo), "v"(hi)); return r;
}
__device__ __forceinline__ float rbf(float f) { return bflo(cvtpk(f, f)); }
__device__ __forceinline__ float dot2(u32 a, u32 b, float c) {
  return __builtin_amdgcn_fdot2_f32_bf16(__builtin_bit_cast(bf16x2, a), __builtin_bit_cast(bf16x2, b), c, false);
}
template <int CTRL> __device__ __forceinline__ float dpp(float x) {
  return __builtin_bit_cast(float, __builtin_amdgcn_mov_dpp(__builtin_bit_cast(int, x), CTRL, 0xf, 0xf, true));
}
constexpr int XOR1 = 0xB1, XOR2 = 0x4E, HMIR = 0x141, MIR = 0x140, ROR8 = 0x128;
__device__ __forceinline__ float sum16(float x) {
  x += dpp<XOR1>(x); x += dpp<XOR2>(x); x += dpp<HMIR>(x); x += dpp<MIR>(x); return x;
}
__device__ __forceinline__ float max16(float x) {
  x = fmaxf(x, dpp<XOR1>(x)); x = fmaxf(x, dpp<XOR2>(x)); x = fmaxf(x, dpp<HMIR>(x)); x = fmaxf(x, dpp<MIR>(x)); return x;
}
__device__ __forceinline__ float x16sum(float x) {
  auto s = __builtin_amdgcn_permlane16_swap(__float_as_uint(x), __float_as_uint(x), false, false);
  return __uint_as_float(s[0]) + __uint_as_float(s[1]);
}
__device__ __forceinline__ float x32sum(float x) {
  auto s = __builtin_amdgcn_permlane32_swap(__float_as_uint(x), __float_as_uint(x), false, false);
  return __uint_as_float(s[0]) + __uint_as_float(s[1]);
}
__device__ __forceinline__ float x16max(float x) {
  auto s = __builtin_amdgcn_permlane16_swap(__float_as_uint(x), __float_as_uint(x), false, false);
  return fmaxf(__uint_as_float(s[0]), __uint_as_float(s[1]));
}
__device__ __forceinline__ float x32max(float x) {
  auto s = __builtin_amdgcn_permlane32_swap(__float_as_uint(x), __float_as_uint(x), false, false);
  return fmaxf(__uint_as_float(s[0]), __uint_as_float(s[1]));
}
__device__ __forceinline__ float sum32(float x) { return x16sum(sum16(x)); }
__device__ __forceinline__ float sum64(float x) { return x32sum(x16sum(sum16(x))); }
__device__ __forceinline__ float max64(float x) { return x32max(x16max(max16(x))); }
#define SB() __builtin_amdgcn_sched_barrier(0)
template <int I> struct IC { static constexpr int value = I; };
template <int B, int E, typename Fn> __device__ __forceinline__ void static_for(Fn&& f) {
  if constexpr (B < E) { f(IC<B>{}); static_for<B + 1, E>(f); }
}
__device__ __forceinline__ void lds_wait() { asm volatile("s_waitcnt lgkmcnt(0)" ::: "memory"); }
__device__ __forceinline__ void wg_barrier() { lds_wait(); __builtin_amdgcn_s_barrier(); asm volatile("" ::: "memory"); }
__device__ __forceinline__ void put(gu64* g, u32 tag, u32 v) { __hip_atomic_store(g, ((u64)tag << 32) | v, RLX_AGENT); }
__device__ __forceinline__ u64 get(gu64* g) { return __hip_atomic_load(g, RLX_AGENT); }

__device__ __forceinline__ float treduce32(float (&pv)[32], int lane) {
#pragma unroll
  for (int j = 0; j < 16; ++j) {
    auto t = __builtin_amdgcn_permlane32_swap(__float_as_uint(pv[j]), __float_as_uint(pv[j + 16]), false, false);
    pv[j] = __uint_as_float(t[0]) + __uint_as_float(t[1]);
  }
#pragma unroll
  for (int j = 0; j < 8; ++j) {
    auto t = __builtin_amdgcn_permlane16_swap(__float_as_uint(pv[j]), __float_as_uint(pv[j + 8]), false, false);
    pv[j] = __uint_as_float(t[0]) + __uint_as_float(t[1]);
  }
  float mine = 0.f;
#pragma unroll
  for (int j = 0; j < 8; ++j) { const float t = sum16(pv[j]); mine = ((lane & 15) == j) ? t : mine; }
  return mine;
}

#define ST(k)
#define STT(k)

constexpr int L_Q = 0;
constexpr int L_TAG = L_Q + 128;
constexpr int L_XN = L_TAG + 4;
constexpr int L_P = L_XN + 448;
constexpr int L_O = L_P + 1024;
constexpr int L_ML = L_O + NW * 256;
constexpr int L_ON = L_ML + 16;
constexpr int L_YP = L_ON + 32;
constexpr int L_H = L_YP + NW * D;
constexpr int L_RED = L_H + 16;
constexpr int L_END = L_RED + 8;
__device__ __forceinline__ void lds_spin(float* lds, int idx, u32 want) {
  volatile LAS u32* p = (volatile LAS u32*)(lds + idx);
#pragma unroll 1
  for (unsigned spin = 0; spin < (SPIN_CAP << 4); ++spin) {
    if ((u32)__builtin_amdgcn_readfirstlane(*p) >= want) break;
    __builtin_amdgcn_s_sleep(2);
  }
  asm volatile("" ::: "memory");
}
__device__ __forceinline__ float sum8(float x) { x += dpp<XOR1>(x); x += dpp<XOR2>(x); x += dpp<HMIR>(x); return x; }
__device__ __forceinline__ float max8(float x) { x = fmaxf(x, dpp<XOR1>(x)); x = fmaxf(x, dpp<XOR2>(x)); x = fmaxf(x, dpp<HMIR>(x)); return x; }

__device__ __forceinline__ u32x4 layernorm8(const u32x4 xr, int lane) {
  float v[8];
#pragma unroll
  for (int j = 0; j < 4; ++j) { v[2 * j] = bflo(xr[j]); v[2 * j + 1] = bfhi(xr[j]); }
  float sm = ((v[0] + v[1]) + (v[2] + v[3])) + ((v[4] + v[5]) + (v[6] + v[7]));
  sm = lane < 56 ? sm : 0.f;
  const float mean = sum64(sm) * (1.0f / D);
  float sq = 0.f;
#pragma unroll
  for (int j = 0; j < 8; ++j) { const float d = v[j] - mean; sq += d * d; }
  sq = lane < 56 ? sq : 0.f;
  const float var = rbf(sum64(sq) * (1.0f / D));
  const float rden = 1.0f / rbf(sqrtf(rbf(var + 1e-5f)));
  const float mb = rbf(mean);
  u32x4 o;
#pragma unroll
  for (int j = 0; j < 4; ++j) {
    const u32 d = cvtpk(v[2 * j] - mb, v[2 * j + 1] - mb);
    o[j] = cvtpk(bflo(d) * rden, bfhi(d) * rden);
  }
  return o;
}
template <int NP, int OFF, int N>
__device__ __forceinline__ void gemv_pairs(const u32x4 (&t)[2 * NP], const u32 (&xa)[N], const u32 (&xb)[N], float (&pa)[8], float (&pb)[8]) {
#pragma unroll
  for (int i = 0; i < NP; ++i) {
    const u32 x0 = xa[OFF + i], x1 = xb[OFF + i];
#pragma unroll
    for (int j = 0; j < 4; ++j) {
      const u32 lo = __builtin_amdgcn_perm(t[2 * i + 1][j], t[2 * i][j], 0x05040100u), hi = __builtin_amdgcn_perm(t[2 * i + 1][j], t[2 * i][j], 0x07060302u);
      pa[2 * j] = dot2(lo, x0, pa[2 * j]); pa[2 * j + 1] = dot2(hi, x0, pa[2 * j + 1]);
      pb[2 * j] = dot2(lo, x1, pb[2 * j]); pb[2 * j + 1] = dot2(hi, x1, pb[2 * j + 1]);
    }
  }
}
__device__ __forceinline__ void reduce_rg(float (&pa)[8], float (&pb)[8], int lane, float& r0, float& r1) {
#pragma unroll
  for (int j = 0; j < 8; ++j) {
    auto t = __builtin_amdgcn_permlane32_swap(__float_as_uint(pa[j]), __float_as_uint(pb[j]), false, false);
    pa[j] = __uint_as_float(t[0]) + __uint_as_float(t[1]);
  }
#pragma unroll
  for (int j = 0; j < 4; ++j) {
    auto t = __builtin_amdgcn_permlane16_swap(__float_as_uint(pa[j]), __float_as_uint(pa[j + 4]), false, false);
    pa[j] = __uint_as_float(t[0]) + __uint_as_float(t[1]);
  }
  float a0 = pa[0] + dpp<ROR8>(pa[0]), a1 = pa[1] + dpp<ROR8>(pa[1]), a2 = pa[2] + dpp<ROR8>(pa[2]), a3 = pa[3] + dpp<ROR8>(pa[3]);
  asm volatile("" : "+v"(a0), "+v"(a1), "+v"(a2), "+v"(a3));
  const bool bit3 = (lane >> 3) & 1;
  r0 = bit3 ? a2 : a0; r1 = bit3 ? a3 : a1;
}

__global__ void __launch_bounds__(NT, 3) main_k(const u16* __restrict__ x, const u32x4* __restrict__ Kc, const u32x4* __restrict__ Vc,
                                            const u16* __restrict__ wq, const u16* __restrict__ wo, const u16* __restrict__ wg,
                                            const u16* __restrict__ wu, const u16* __restrict__ wd, u64* ws, u16* __restrict__ out) {
  __shared__ __attribute__((aligned(16))) float lds[L_END];
  gu64* const G = (gu64*)ws + (size_t)GOFF * 131072;
  const int lane = threadIdx.x & 63, half = lane >> 5, l32 = lane & 31;
  const int wv = __builtin_amdgcn_readfirstlane(threadIdx.x >> 6);
  const int bid = blockIdx.x;
  const int slot = MAPX == 0 ? bid : (bid & 7) * 32 + (bid >> 3);
  const int g = slot >> 3, s = slot & 7, b = g >> 4, h = g & 15;
  const int rg = lane >> 3, l56 = lane < 56 ? lane : 55;

  if (wv < 2) {
    ST(0);
    if (wv == 1 && g >= 28) {
      __builtin_amdgcn_s_sleep(127);
      if (PRET) {
        const int ib = h * 8 + s;
        const GAS char* pt = lane == 0 ? (const GAS char*)wo + (size_t)(h * DH + s * 32) * (D * 2) : lane == 1 ? (const GAS char*)wd + (size_t)((ib < 112 ? ib : 0) * 16) * (D * 2)
                           : lane == 2 ? (const GAS char*)out : lane == 3 ? (const GAS char*)(G + G_A + slot * 256) : lane == 4 ? (const GAS char*)(G + G_Y + slot * D)
                           : (const GAS char*)Vc + (size_t)slot * (1024 * 512);
        u32 d0 = __builtin_nontemporal_load((const GAS u32*)pt);
        asm volatile("" :: "v"(d0));
      }
      return;
    }
    const u32 tstart = (u32)__builtin_amdgcn_s_memrealtime();
    __builtin_amdgcn_s_setprio(3);
    const int j16 = b * 8 + s, qc = j16 & 3, qk = j16 >> 2;
    const u32 epoch = __hip_atomic_load((gu32*)(G + G_E), RLX_AGENT);
    const u32x4 xr0 = __builtin_nontemporal_load(reinterpret_cast<const u32x4*>(x + l56 * 8));
    const u32x4 xr1 = __builtin_nontemporal_load(reinterpret_cast<const u32x4*>(x + D + l56 * 8));
    const int rr = rg < 7 ? rg : 6;
    const int row0 = (wv ? s * 56 : qk * 112) + rr * 8, row1 = wv ? row0 : row0 + 56;
    const int ld8 = wv ? F / 8 : H * DH / 8, col8 = (wv ? g * 8 : h * 32 + qc * 8) + (lane & 7);
    const u32x4* p0 = reinterpret_cast<const u32x4*>(wv ? wg : wq) + (size_t)row0 * ld8 + col8;
    const u32x4* p1 = reinterpret_cast<const u32x4*>(wv ? wu : wq) + (size_t)row1 * ld8 + col8;
    u32x4 t0[8], t1[8];
#pragma unroll
    for (int i = 0; i < 8; ++i) t0[i] = __builtin_nontemporal_load(p0 + i * ld8);
#pragma unroll
    for (int i = 0; i < 8; ++i) t1[i] = __builtin_nontemporal_load(p1 + i * ld8);
    SB();
    const u32x4 n0 = layernorm8(xr0, lane), n1 = layernorm8(xr1, lane);
    const u32 tag = __builtin_amdgcn_readfirstlane(epoch) + 0x9E3779B9u;
    float pa[8], pb[8], r0, r1;
#pragma unroll
    for (int j = 0; j < 8; ++j) { pa[j] = 0.f; pb[j] = 0.f; }
    gu64* po = wv ? G + G_F + ((g * 2 + half) * 8 + s) * 128 + l32 : G + G_Q + ((h * 2 + half) * 4 + qk) * 256 + qc * 64 + l32;
#pragma unroll 1
    for (int blk = 0; blk < 2; ++blk) {
      const int src = ((blk ? row1 : row0) >> 3) * 4;
      u32 xa[4], xb[4];
#pragma unroll
      for (int i = 0; i < 4; ++i) {
        const u32 ta = __builtin_amdgcn_ds_bpermute(src, n0[i]), tb = __builtin_amdgcn_ds_bpermute(src, n1[i]);
        xa[i] = rg < 7 ? ta : 0u; xb[i] = rg < 7 ? tb : 0u;
      }
      gemv_pairs<4, 0, 4>(t0, xa, xb, pa, pb);
#pragma unroll
      for (int i = 0; i < 8; ++i) t0[i] = t1[i];
      if (wv || blk) {
        reduce_rg(pa, pb, lane, r0, r1);
        put(po, tag, __float_as_uint(r0)); put(po + 32, tag, __float_as_uint(r1));
        po += 64;
#pragma unroll
        for (int j = 0; j < 8; ++j) { pa[j] = 0.f; pb[j] = 0.f; }
      }
    }
    if (wv) {
      if (PRET) {
        const int ib = h * 8 + s;
        const GAS char* pt = lane == 0 ? (const GAS char*)wo + (size_t)(h * DH + s * 32) * (D * 2) : lane == 1 ? (const GAS char*)wd + (size_t)((ib < 112 ? ib : 0) * 16) * (D * 2)
                           : lane == 2 ? (const GAS char*)out : lane == 3 ? (const GAS char*)(G + G_A + slot * 256) : lane == 4 ? (const GAS char*)(G + G_Y + slot * D)
                           : (const GAS char*)Vc + (size_t)slot * (1024 * 512);
        u32 d0 = __builtin_nontemporal_load((const GAS u32*)pt);
        asm volatile("" :: "v"(d0));
      }
      return;
    }
    if (lane == 0) put(G + G_T + h * 16 + j16, tag, tstart);
    __builtin_amdgcn_s_sleep(QDELAY);
    ST(1);
    {
      gu64* qg = G + G_Q + (h * 2 + b) * 1024 + half * 128 + l32;
      float qs[4]; u32 tv = 0;
#pragma unroll 1
      for (unsigned spin = 0; spin < SPIN_CAP; ++spin) {
        const u64 tt = get(G + G_T + h * 16 + (lane & 15));
        tv = (u32)tt;
        if (!__all((u32)(tt >> 32) == tag)) { __builtin_amdgcn_s_sleep(4); continue; }
        u64 v[16];
#pragma unroll
        for (int k = 0; k < 4; ++k)
#pragma unroll
          for (int j = 0; j < 4; ++j) v[k * 4 + j] = get(qg + k * 256 + j * 32);
        bool ok = true;
#pragma unroll
        for (int k = 0; k < 16; ++k) ok &= (u32)(v[k] >> 32) == tag;
#pragma unroll
        for (int j = 0; j < 4; ++j) qs[j] = (__uint_as_float((u32)v[j]) + __uint_as_float((u32)v[4 + j])) + (__uint_as_float((u32)v[8 + j]) + __uint_as_float((u32)v[12 + j]));
        if (__all(ok)) break;
      }
      const int pr = (l32 & 7) * 4 + ((l32 >> 4) & 1) * 2 + ((l32 >> 3) & 1);
      reinterpret_cast<u32*>(lds + L_Q)[(half * 2) * 32 + pr] = cvtpk(qs[0], qs[1]);
      reinterpret_cast<u32*>(lds + L_Q)[(half * 2 + 1) * 32 + pr] = cvtpk(qs[2], qs[3]);
      const float early = -max16((float)(int)(tstart - tv));
      if (lane == 0) { reinterpret_cast<u32*>(lds)[L_TAG] = tag; reinterpret_cast<u32*>(lds)[L_TAG + 1] = tstart + (u32)(int)early; reinterpret_cast<u32*>(lds)[L_TAG + 2] = 0u; }
    }
    ST(2);
    wg_barrier();
    return;
  }

  const int w = wv - 2, tid = threadIdx.x - 128;
  constexpr int GW = KMODE == 0 ? 8 : KMODE == 2 ? 4 : KMODE == 3 ? 2 : 1, KST = GW * 1024;
  const int woff = (w / GW) * (GW * 65536) + (w % GW) * 1024;
  u32x4 buf[WIN];
  const GAS char* lp = (const GAS char*)Kc + (size_t)slot * (1024 * 512) + woff;
  u32 loff = lane * 16;
  long long st_n = KST, st_3 = KST, st_5 = KST;
  int lag = 0; u32 tref = 0;
  auto pace = [&](int n) {
    if (FB) {
      lag = __builtin_amdgcn_readfirstlane((int)((u32)__builtin_amdgcn_s_memrealtime() - tref) - (FB_OFF + n * FB_TOT / 128));
    }
  };
  auto issue = [&](auto jc) {
    constexpr int j = decltype(jc)::value;
    asm volatile("s_waitcnt vmcnt(%1)\n\ts_cmpk_gt_i32 %0, %4\n\ts_cbranch_scc1 1f\n\ts_waitcnt vmcnt(%2)\n\ts_cmpk_lt_i32 %0, %5\n\ts_cbranch_scc0 1f\n\ts_waitcnt vmcnt(%3)\n1:"
                 :: "s"(lag), "i"(DH_ - 1), "i"(INFL - 1), "i"(DL - 1), "i"(FB_BAND), "i"(-FB_BAND) : "memory", "scc");
    asm volatile("" : "+s"(lp));
    buf[j] = __builtin_nontemporal_load((const GAS u32x4*)(lp + loff));
    lp += j == 3 ? st_3 : (j >= 5 && j < RING) ? st_5 : st_n;
  };
  static_for<0, WIN>([&](auto jc) {
    constexpr int j = decltype(jc)::value;
    asm volatile("s_waitcnt vmcnt(%0)" :: "i"(INFL - 1) : "memory");
    asm volatile("" : "+s"(lp));
    buf[j] = __builtin_nontemporal_load((const GAS u32x4*)(lp + loff));
    lp += KST;
  });
  SB();
  ST(3);
  wg_barrier();
  const u32x4 qp = *reinterpret_cast<const u32x4*>(lds + L_Q + l32 * 4);
  const u32 tag = __builtin_amdgcn_readfirstlane(reinterpret_cast<const u32*>(lds)[L_TAG]);
  tref = __builtin_amdgcn_readfirstlane(reinterpret_cast<const u32*>(lds)[L_TAG + 1]);
  ST(4);

  float* const pw = lds + L_P + (w * 2 + half) * NLD;
  float sr = 0.f, s0 = 0.f;
  pace(WIN);
  static_for<0, WIN>([&](auto jc) {
    constexpr int j = decltype(jc)::value;
    const u32x4 kv = buf[j];
    float sacc = dot2(kv[0], qp[0], 0.f);
    sacc = dot2(kv[1], qp[1], sacc); sacc = dot2(kv[2], qp[2], sacc); sacc = dot2(kv[3], qp[3], sacc);
    asm volatile("" : "+v"(sacc));
    if constexpr ((j & 3) == 0) issue(IC<j / 4>{});
    sacc = sum32(sacc);
    s0 = (l32 == j) ? sacc : s0;
    asm volatile("" : "+v"(s0));
    SB();
  });
#pragma unroll 1
  for (int it = 0; it < 4; ++it) {
    if (it == 3) lp = (const GAS char*)Vc + (size_t)slot * (1024 * 512) + woff;
    pace(40 + it * 8);
    const int lj = l32 - it * 8;
    static_for<0, RING>([&](auto jc) {
      constexpr int j = decltype(jc)::value;
      const u32x4 kv = buf[j];
      float sacc = dot2(kv[0], qp[0], 0.f);
      sacc = dot2(kv[1], qp[1], sacc); sacc = dot2(kv[2], qp[2], sacc); sacc = dot2(kv[3], qp[3], sacc);
      asm volatile("" : "+v"(sacc));
      issue(jc);
      sacc = sum32(sacc);
      sr = (lj == j) ? sacc : sr;
      SB();
    });
  }
  ST(5);
  {
    const float a = rbf(s0) * C2, c = rbf(sr) * C2;
    const float mx = max64(fmaxf(a, c));
    const float p0 = __builtin_amdgcn_exp2f(a - mx), p1 = __builtin_amdgcn_exp2f(c - mx);
    pw[l32] = p0; pw[32 + l32] = p1;
    const float ls = sum64(p0 + p1);
    if (lane == 0) { lds[L_ML + 2 * w] = mx; lds[L_ML + 2 * w + 1] = ls; }
    lds_wait();
    if (lane == 0) __hip_atomic_fetch_add((LAS u32*)(lds + L_TAG + 2), 1u, __ATOMIC_RELAXED, __HIP_MEMORY_SCOPE_WORKGROUP);
  }
  u16 xv = 0;
  constexpr int NCONS = YMODE == 2 ? 56 : 224, CCOL = 896 / NCONS;
  if (bid < NCONS && tid < CCOL) xv = x[bid * CCOL + tid];
  lds_wait();
  SB();

  f32x2 ac[4];
#pragma unroll
  for (int j = 0; j < 4; ++j) ac[j] = f32x2{0.f, 0.f};
  float wscale = 1.f, Mwg = 0.f, Lwg = 0.f;
#pragma unroll 1
  for (int it = 0; it < 8; ++it) {
    pace(72 + it * 8);
    if (EARLY && it == 6) {
      lds_spin(lds, L_TAG + 2, 8u);
      const f32x2 ml = *reinterpret_cast<const f32x2*>(lds + L_ML + 2 * (lane & 7));
      Mwg = max8(ml[0]);
      const float e = __builtin_amdgcn_exp2f(ml[0] - Mwg);
      Lwg = sum8(e * ml[1]);
      wscale = __uint_as_float(__builtin_amdgcn_readlane(__float_as_uint(e), w));
    }
    if (CPF && it == CPF && w == 0) {
      u64 pc; asm volatile("s_getpc_b64 %0" : "=s"(pc));
      const GAS u32* cp = (const GAS u32*)(pc & ~127ull) + (lane < 36 ? lane : 35) * 32;
      u32 d0 = cp[0];
      asm volatile("" :: "v"(d0));
    }
    if (it == 7) {
      const int ib = h * 8 + s;
      lp = (const GAS char*)wo + (size_t)(h * DH + s * 32 + 4 * w) * (D * 2);
      const GAS char* wdp = (const GAS char*)wd + (size_t)((ib < 112 ? ib : 0) * 16 + 2 * w) * (D * 2);
      loff = l56 * 16; st_n = D * 2; st_3 = wdp - (lp + 3 * D * 2); st_5 = 0; lag = 10000;
    }
    f32x4 pr;
    static_for<0, RING>([&](auto jc) {
      constexpr int j = decltype(jc)::value;
      if constexpr ((j & 3) == 0) pr = *reinterpret_cast<const f32x4*>(pw + it * 8 + j);
      const u32x4 vv = buf[j];
      const float p = pr[j & 3];
#pragma unroll
      for (int k = 0; k < 4; ++k) ac[k] = __builtin_elementwise_fma(f32x2{p, p}, f32x2{bflo(vv[k]), bfhi(vv[k])}, ac[k]);
      asm volatile("" : "+v"(ac[0]), "+v"(ac[1]), "+v"(ac[2]), "+v"(ac[3]));
      issue(jc);
      SB();
    });
  }
  ST(9);

  {
    float a[8];
#pragma unroll
    for (int j = 0; j < 4; ++j) { a[2 * j] = x32sum(ac[j][0]) * wscale; a[2 * j + 1] = x32sum(ac[j][1]) * wscale; }
    *reinterpret_cast<f32x4*>(lds + L_O + w * 256 + l32 * 8 + half * 4) = half ? f32x4{a[4], a[5], a[6], a[7]} : f32x4{a[0], a[1], a[2], a[3]};
    wg_barrier();
    if (tid < 256) {
      float M = Mwg, L = Lwg, o = 0.f;
      if (EARLY) {
#pragma unroll
        for (int ww = 0; ww < NW; ++ww) o += lds[L_O + ww * 256 + tid];
      } else {
        M = -INFINITY; L = 0.f;
#pragma unroll
        for (int ww = 0; ww < NW; ++ww) M = fmaxf(M, lds[L_ML + 2 * ww]);
#pragma unroll
        for (int ww = 0; ww < NW; ++ww) {
          const float sc = __builtin_amdgcn_exp2f(lds[L_ML + 2 * ww] - M);
          L = fmaf(sc, lds[L_ML + 2 * ww + 1], L);
          o = fmaf(sc, lds[L_O + ww * 256 + tid], o);
        }
      }
      put(G + G_A + slot * 256 + tid, tag, __float_as_uint(o));
      if (tid < 2) put(G + G_ST + slot * 2 + tid, tag, __float_as_uint(tid ? L : M));
      if (w == 3) {
        float hv = 0.f;
        const int ib = h * 8 + s;
        if (ib < 112) {
          const int col = (ib & 3) * 16 + (lane & 15);
          const int pos = (col & 1) * 32 + (col >> 3) + ((col >> 2) & 1) * 16 + ((col >> 1) & 1) * 8;
          gu64* fg = G + G_F + ((((ib >> 2) * 2 + b) * 8 + half * 4) * 2 + ((lane >> 4) & 1)) * 64 + pos;
          float sum = 0.f;
#pragma unroll 1
          for (unsigned spin = 0; spin < SPIN_CAP; ++spin) {
            bool ok = true; sum = 0.f;
#pragma unroll
            for (int k = 0; k < 4; ++k) { const u64 v = get(fg + k * 128); sum += __uint_as_float((u32)v); ok &= (u32)(v >> 32) == tag; }
            if (__all(ok)) break;
            __builtin_amdgcn_s_sleep(8);
          }
          sum = rbf(x32sum(sum));
          auto sw = __builtin_amdgcn_permlane16_swap(__float_as_uint(sum), __float_as_uint(sum), false, false);
          const float gv = __uint_as_float(sw[0]), uv = __uint_as_float(sw[1]);
          hv = rbf(rbf(gv / (1.f + __expf(-gv))) * uv);
        }
        if (lane < 16) lds[L_H + lane] = hv;
      }
    } else {
      const int t = tid - 256, d = t >> 3, sp = t & 7;
      gu64* pa = G + G_A + (g * 8 + sp) * 256 + s * 32 + d;
      gu64* pm = G + G_ST + (g * 8 + sp) * 2;
      float av = 0.f, mv = 0.f, lv = 0.f;
      if (FD1) __builtin_amdgcn_s_sleep(FD1);
      bool ok = false;
      if (PIPE) {
        u64 a0 = get(pa), m0 = get(pm), l0 = get(pm + 1);
#pragma unroll 1
        for (unsigned spin = 0; spin < SPIN_CAP; ++spin) {
          __builtin_amdgcn_s_sleep(PGAP);
          const u64 a1 = get(pa), m1 = get(pm), l1 = get(pm + 1);
          SB();
          av = __uint_as_float((u32)a0); mv = __uint_as_float((u32)m0); lv = __uint_as_float((u32)l0);
          if (__all((u32)(a0 >> 32) == tag && (u32)(m0 >> 32) == tag && (u32)(l0 >> 32) == tag)) break;
          __builtin_amdgcn_s_sleep(PGAP);
          a0 = get(pa); m0 = get(pm); l0 = get(pm + 1);
          SB();
          av = __uint_as_float((u32)a1); mv = __uint_as_float((u32)m1); lv = __uint_as_float((u32)l1);
          if (__all((u32)(a1 >> 32) == tag && (u32)(m1 >> 32) == tag && (u32)(l1 >> 32) == tag)) break;
        }
      } else {
#pragma unroll 1
        for (unsigned spin = 0; spin < SPIN_CAP; ++spin) {
          if (!ok) {
            const u64 ta = get(pa), tm = get(pm), tl = get(pm + 1);
            av = __uint_as_float((u32)ta); mv = __uint_as_float((u32)tm); lv = __uint_as_float((u32)tl);
            ok = (u32)(ta >> 32) == tag && (u32)(tm >> 32) == tag && (u32)(tl >> 32) == tag;
          }
          if (__all(ok)) break;
          if (PS1) __builtin_amdgcn_s_sleep(PS1);
        }
      }
      const float M = max8(mv);
      const float e = __builtin_amdgcn_exp2f(mv - M);
      const float L = sum8(e * lv), o = sum8(e * av);
      if (sp == 0) lds[L_ON + d] = o / L;
    }
    float y[8];
    {
#pragma unroll
      for (int k = 0; k < 3; ++k) {
        const u32x4 a = buf[2 * k], c = buf[2 * k + 1];
#pragma unroll
        for (int j = 0; j < 4; ++j) { buf[2 * k][j] = __builtin_amdgcn_perm(c[j], a[j], 0x05040100u); buf[2 * k + 1][j] = __builtin_amdgcn_perm(c[j], a[j], 0x07060302u); }
      }
    }
    wg_barrier();
    ST(10);
    {
      const f32x2 hh = *reinterpret_cast<const f32x2*>(lds + L_H + 2 * w);
      const u32 h01 = cvtpk(hh[0], hh[1]);
#pragma unroll
      for (int j = 0; j < 4; ++j) { y[2 * j] = dot2(buf[4][j], h01, 0.f); y[2 * j + 1] = dot2(buf[5][j], h01, 0.f); }
    }
    const f32x4 ov = *reinterpret_cast<const f32x4*>(lds + L_ON + 4 * w);
    const u32 o01 = cvtpk(ov[0], ov[1]), o23 = cvtpk(ov[2], ov[3]);
#pragma unroll
    for (int j = 0; j < 4; ++j) {
      y[2 * j] = dot2(buf[2][j], o23, dot2(buf[0][j], o01, y[2 * j]));
      y[2 * j + 1] = dot2(buf[3][j], o23, dot2(buf[1][j], o01, y[2 * j + 1]));
    }
    if (lane < 56) {
      *reinterpret_cast<f32x4*>(lds + L_YP + w * D + lane * 8) = f32x4{y[0], y[1], y[2], y[3]};
      *reinterpret_cast<f32x4*>(lds + L_YP + w * D + lane * 8 + 4) = f32x4{y[4], y[5], y[6], y[7]};
    }
    wg_barrier();
    if (tid < D) {
      float t = 0.f;
#pragma unroll
      for (int ww = 0; ww < NW; ++ww) t += lds[L_YP + ww * D + tid];
      put(YMODE == 0 ? G + G_Y + ((b * 112 + (tid >> 2)) * 128 + (slot & 127)) * 4 + (tid & 3) : G + G_Y + slot * D + tid, tag, __float_as_uint(t));
    }
  }
  ST(8);

  if (bid < NCONS) {
    if (YMODE < 2) {
      if (w < 2) {
        gu64* src = YMODE == 0 ? G + G_Y + (bid * 128 + tid) * 4 : G + G_Y + ((bid / 112) * 128 + tid) * D + (bid % 112) * 4;
        float v[4] = {0.f, 0.f, 0.f, 0.f};
        bool ok = false;
#pragma unroll 1
        for (unsigned spin = 0; spin < SPIN_CAP; ++spin) {
          if (!ok) {
            ok = true;
#pragma unroll
            for (int k = 0; k < 4; ++k) { const u64 t = get(src + k); v[k] = __uint_as_float((u32)t); ok &= (u32)(t >> 32) == tag; }
          }
          if (__all(ok)) break;
          __builtin_amdgcn_s_sleep(2);
        }
#pragma unroll
        for (int k = 0; k < 4; ++k) v[k] = sum64(v[k]);
        if (lane == 0) *reinterpret_cast<f32x4*>(lds + L_O + w * 4) = f32x4{v[0], v[1], v[2], v[3]};
      }
      wg_barrier();
      if (tid < 4) {
        const float r = __uint_as_float((u32)xv << 16) + (lds[L_O + tid] + lds[L_O + 4 + tid]);
        out[bid * 4 + tid] = (u16)(cvtpk(r, r) & 0xffffu);
      }
    } else {
      gu64* src = G + G_Y + ((bid / 28) * 128 + (tid >> 4) * 4) * D + (bid % 28) * 16 + (tid & 15);
      float v = 0.f;
      if (FD2) __builtin_amdgcn_s_sleep(FD2);
      bool ok = false;
      if (PIPE) {
        u64 t0[4];
#pragma unroll
        for (int k = 0; k < 4; ++k) t0[k] = get(src + k * D);
#pragma unroll 1
        for (unsigned spin = 0; spin < SPIN_CAP; ++spin) {
          __builtin_amdgcn_s_sleep(PGAP);
          u64 t1[4];
#pragma unroll
          for (int k = 0; k < 4; ++k) t1[k] = get(src + k * D);
          SB();
          bool k0 = true; v = 0.f;
#pragma unroll
          for (int k = 0; k < 4; ++k) { v += __uint_as_float((u32)t0[k]); k0 &= (u32)(t0[k] >> 32) == tag; }
          if (__all(k0)) break;
          __builtin_amdgcn_s_sleep(PGAP);
#pragma unroll
          for (int k = 0; k < 4; ++k) t0[k] = get(src + k * D);
          SB();
          k0 = true; v = 0.f;
#pragma unroll
          for (int k = 0; k < 4; ++k) { v += __uint_as_float((u32)t1[k]); k0 &= (u32)(t1[k] >> 32) == tag; }
          if (__all(k0)) break;
        }
      } else {
#pragma unroll 1
        for (unsigned spin = 0; spin < SPIN_CAP; ++spin) {
          if (!ok) {
            ok = true; v = 0.f;
#pragma unroll
            for (int k = 0; k < 4; ++k) { const u64 t = get(src + k * D); v += __uint_as_float((u32)t); ok &= (u32)(t >> 32) == tag; }
          }
          if (__all(ok)) break;
          if (PS1) __builtin_amdgcn_s_sleep(PS1);
        }
      }
      v = x32sum(x16sum(v));
      if (lane < 16) lds[L_O + w * 16 + lane] = v;
      wg_barrier();
      if (tid < 16) {
        float r = 0.f;
#pragma unroll
        for (int ww = 0; ww < NW; ++ww) r += lds[L_O + ww * 16 + tid];
        r += __uint_as_float((u32)xv << 16);
        out[bid * 16 + tid] = (u16)(cvtpk(r, r) & 0xffffu);
      }
    }
  } else if (bid == NWG - 1) {
    if (w < 4) {
      bool ok = false;
#pragma unroll 1
      for (unsigned spin = 0; spin < SPIN_CAP; ++spin) {
        if (!ok) ok = (u32)(get(YMODE == 0 ? G + G_Y + ((tid >> 7) * 112 * 128 + (tid & 127)) * 4 : G + G_Y + tid * D) >> 32) == tag;
        if (__all(ok)) break;
        __builtin_amdgcn_s_sleep(2);
      }
    }
    wg_barrier();
    if (tid == 0) __hip_atomic_store((gu32*)(G + G_E), tag, RLX_AGENT);
  }
  ST(11);
}

extern "C" void kernel_launch(void* const* d_in, const int* in_sizes, int n_in,
                              void* d_out, int out_size, void* d_ws, size_t ws_size,
                              hipStream_t stream) {
  main_k<<<NWG, NT, 0, stream>>>((const u16*)d_in[0], (const u32x4*)d_in[1], (const u32x4*)d_in[2], (const u16*)d_in[3],
                                 (const u16*)d_in[4], (const u16*)d_in[6], (const u16*)d_in[7], (const u16*)d_in[8],
                                 (u64*)d_ws, (u16*)d_out);
}
```

```cpp
#include <hip/hip_runtime.h>
#include <hip/hip_bf16.h>
#include <stdint.h>

typedef unsigned int u32;
typedef unsigned short u16;
typedef unsigned long long u64;
typedef __attribute__((ext_vector_type(4))) unsigned u32x4;
typedef __attribute__((ext_vector_type(4))) float f32x4;
typedef __attribute__((ext_vector_type(2))) float f32x2;
typedef __attribute__((ext_vector_type(2))) __bf16 bf16x2;
#define GAS __attribute__((address_space(1)))
typedef GAS u32 gu32;
typedef GAS u64 gu64;
#define RLX_AGENT __ATOMIC_RELAXED, __HIP_MEMORY_SCOPE_AGENT

constexpr int D = 448, H = 16, DH = 256, F = 1792;
constexpr int NWG = 256, NW = 8, NT = (NW + 2) * 64;
constexpr int RING = 8, WIN = 32, NLD = 64;
constexpr int IF_LO = 2, IF_MID = 4, IF_HI = 6;
constexpr int FB_OFF = 150, FB_TOT = 3630, FB_BAND = 50;
constexpr int NCONS = 56, CCOL = 16;
constexpr unsigned SPIN_CAP = 1u << 18;
constexpr float C2 = 0.0625f * 1.4426950408889634f;

constexpr int G_E = 0;
constexpr int G_T = 32;
constexpr int G_Q = G_T + 256;
constexpr int G_F = G_Q + 32 * 1024;
constexpr int G_ST = G_F + 28 * 2 * 8 * 128;
constexpr int G_A = G_ST + 32 * 16;
constexpr int G_Y = G_A + 256 * 256;

constexpr int L_Q = 0;
constexpr int L_TAG = L_Q + 128;
constexpr int L_P = L_TAG + 4;
constexpr int L_O = L_P + 1024;
constexpr int L_ML = L_O + NW * 256;
constexpr int L_ON = L_ML + 16;
constexpr int L_YP = L_ON + 32;
constexpr int L_H = L_YP + NW * D;
constexpr int L_END = L_H + 16;

__device__ __forceinline__ float bflo(u32 v) { return __uint_as_float(v << 16); }
__device__ __forceinline__ float bfhi(u32 v) { return __uint_as_float(v & 0xffff0000u); }
__device__ __forceinline__ u32 cvtpk(float lo, float hi) {
  return __builtin_bit_cast(u32, __builtin_convertvector(f32x2{lo, hi}, bf16x2));
}
__device__ __forceinline__ float rbf(float f) { return bflo(cvtpk(f, f)); }
__device__ __forceinline__ float dot2(u32 a, u32 b, float c) {
  return __builtin_amdgcn_fdot2_f32_bf16(__builtin_bit_cast(bf16x2, a), __builtin_bit_cast(bf16x2, b), c, false);
}
template <int CTRL> __device__ __forceinline__ float dpp(float x) {
  return __builtin_bit_cast(float, __builtin_amdgcn_mov_dpp(__builtin_bit_cast(int, x), CTRL, 0xf, 0xf, true));
}
constexpr int XOR1 = 0xB1, XOR2 = 0x4E, HMIR = 0x141, MIR = 0x140, ROR8 = 0x128;
__device__ __forceinline__ float sum8(float x) { x += dpp<XOR1>(x); x += dpp<XOR2>(x); x += dpp<HMIR>(x); return x; }
__device__ __forceinline__ float max8(float x) { x = fmaxf(x, dpp<XOR1>(x)); x = fmaxf(x, dpp<XOR2>(x)); x = fmaxf(x, dpp<HMIR>(x)); return x; }
__device__ __forceinline__ float sum16(float x) { x = sum8(x); return x + dpp<MIR>(x); }
__device__ __forceinline__ float max16(float x) { x = max8(x); return fmaxf(x, dpp<MIR>(x)); }
__device__ __forceinline__ float x16sum(float x) {
  auto s = __builtin_amdgcn_permlane16_swap(__float_as_uint(x), __float_as_uint(x), false, false);
  return __uint_as_float(s[0]) + __uint_as_float(s[1]);
}
__device__ __forceinline__ float x32sum(float x) {
  auto s = __builtin_amdgcn_permlane32_swap(__float_as_uint(x), __float_as_uint(x), false, false);
  return __uint_as_float(s[0]) + __uint_as_float(s[1]);
}
__device__ __forceinline__ float x16max(float x) {
  auto s = __builtin_amdgcn_permlane16_swap(__float_as_uint(x), __float_as_uint(x), false, false);
  return fmaxf(__uint_as_float(s[0]), __uint_as_float(s[1]));
}
__device__ __forceinline__ float x32max(float x) {
  auto s = __builtin_amdgcn_permlane32_swap(__float_as_uint(x), __float_as_uint(x), false, false);
  return fmaxf(__uint_as_float(s[0]), __uint_as_float(s[1]));
}
__device__ __forceinline__ float sum32(float x) { return x16sum(sum16(x)); }
__device__ __forceinline__ float sum64(float x) { return x32sum(x16sum(sum16(x))); }
__device__ __forceinline__ float max64(float x) { return x32max(x16max(max16(x))); }
#define SB() __builtin_amdgcn_sched_barrier(0)
template <int I> struct IC { static constexpr int value = I; };
template <int B, int E, typename Fn> __device__ __forceinline__ void static_for(Fn&& f) {
  if constexpr (B < E) { f(IC<B>{}); static_for<B + 1, E>(f); }
}
__device__ __forceinline__ void lds_wait() { asm volatile("s_waitcnt lgkmcnt(0)" ::: "memory"); }
__device__ __forceinline__ void wg_barrier() { lds_wait(); __builtin_amdgcn_s_barrier(); asm volatile("" ::: "memory"); }
__device__ __forceinline__ void put(gu64* g, u32 tag, u32 v) { __hip_atomic_store(g, ((u64)tag << 32) | v, RLX_AGENT); }
__device__ __forceinline__ u64 get(gu64* g) { return __hip_atomic_load(g, RLX_AGENT); }

#define ST(k)

__device__ __forceinline__ u32x4 layernorm8(const u32x4 xr, int lane) {
  float v[8];
#pragma unroll
  for (int j = 0; j < 4; ++j) { v[2 * j] = bflo(xr[j]); v[2 * j + 1] = bfhi(xr[j]); }
  float sm = ((v[0] + v[1]) + (v[2] + v[3])) + ((v[4] + v[5]) + (v[6] + v[7]));
  sm = lane < 56 ? sm : 0.f;
  const float mean = sum64(sm) * (1.0f / D);
  float sq = 0.f;
#pragma unroll
  for (int j = 0; j < 8; ++j) { const float d = v[j] - mean; sq += d * d; }
  sq = lane < 56 ? sq : 0.f;
  const float var = rbf(sum64(sq) * (1.0f / D));
  const float rden = 1.0f / rbf(sqrtf(rbf(var + 1e-5f)));
  const float mb = rbf(mean);
  u32x4 o;
#pragma unroll
  for (int j = 0; j < 4; ++j) {
    const u32 d = cvtpk(v[2 * j] - mb, v[2 * j + 1] - mb);
    o[j] = cvtpk(bflo(d) * rden, bfhi(d) * rden);
  }
  return o;
}
__device__ __forceinline__ void gemv8(const u32x4 (&t)[8], const u32 (&xa)[4], const u32 (&xb)[4], float (&pa)[8], float (&pb)[8]) {
#pragma unroll
  for (int i = 0; i < 4; ++i)
#pragma unroll
    for (int j = 0; j < 4; ++j) {
      const u32 lo = __builtin_amdgcn_perm(t[2 * i + 1][j], t[2 * i][j], 0x05040100u), hi = __builtin_amdgcn_perm(t[2 * i + 1][j], t[2 * i][j], 0x07060302u);
      pa[2 * j] = dot2(lo, xa[i], pa[2 * j]); pa[2 * j + 1] = dot2(hi, xa[i], pa[2 * j + 1]);
      pb[2 * j] = dot2(lo, xb[i], pb[2 * j]); pb[2 * j + 1] = dot2(hi, xb[i], pb[2 * j + 1]);
    }
}
__device__ __forceinline__ void reduce_rg(float (&pa)[8], float (&pb)[8], int lane, float& r0, float& r1) {
#pragma unroll
  for (int j = 0; j < 8; ++j) {
    auto t = __builtin_amdgcn_permlane32_swap(__float_as_uint(pa[j]), __float_as_uint(pb[j]), false, false);
    pa[j] = __uint_as_float(t[0]) + __uint_as_float(t[1]);
  }
#pragma unroll
  for (int j = 0; j < 4; ++j) {
    auto t = __builtin_amdgcn_permlane16_swap(__float_as_uint(pa[j]), __float_as_uint(pa[j + 4]), false, false);
    pa[j] = __uint_as_float(t[0]) + __uint_as_float(t[1]);
  }
  float a0 = pa[0] + dpp<ROR8>(pa[0]), a1 = pa[1] + dpp<ROR8>(pa[1]), a2 = pa[2] + dpp<ROR8>(pa[2]), a3 = pa[3] + dpp<ROR8>(pa[3]);
  asm volatile("" : "+v"(a0), "+v"(a1), "+v"(a2), "+v"(a3));
  const bool bit3 = (lane >> 3) & 1;
  r0 = bit3 ? a2 : a0; r1 = bit3 ? a3 : a1;
}

__global__ void __launch_bounds__(NT, 3) fused_decode_k(const u16* __restrict__ x, const u32x4* __restrict__ Kc, const u32x4* __restrict__ Vc,
                                                       const u16* __restrict__ wq, const u16* __restrict__ wo, const u16* __restrict__ wg,
                                                       const u16* __restrict__ wu, const u16* __restrict__ wd, u64* ws, u16* __restrict__ out) {
  __shared__ __attribute__((aligned(16))) float lds[L_END];
  gu64* const G = (gu64*)ws;
  const int lane = threadIdx.x & 63, half = lane >> 5, l32 = lane & 31;
  const int wv = __builtin_amdgcn_readfirstlane(threadIdx.x >> 6);
  const int slot = blockIdx.x;
  const int g = slot >> 3, s = slot & 7, b = g >> 4, h = g & 15;
  const int rg = lane >> 3, l56 = lane < 56 ? lane : 55;

  if (wv < 2) {
    ST(0);
    if (wv == 1 && g >= 28) return;
    const u32 tstart = (u32)__builtin_amdgcn_s_memrealtime();
    __builtin_amdgcn_s_setprio(3);
    const int j16 = b * 8 + s, qc = j16 & 3, qk = j16 >> 2;
    const u32 epoch = __hip_atomic_load((gu32*)(G + G_E), RLX_AGENT);
    const u32x4 xr0 = __builtin_nontemporal_load(reinterpret_cast<const u32x4*>(x + l56 * 8));
    const u32x4 xr1 = __builtin_nontemporal_load(reinterpret_cast<const u32x4*>(x + D + l56 * 8));
    const int rr = rg < 7 ? rg : 6;
    const int row0 = (wv ? s * 56 : qk * 112) + rr * 8, row1 = wv ? row0 : row0 + 56;
    const int ld8 = wv ? F / 8 : H * DH / 8, col8 = (wv ? g * 8 : h * 32 + qc * 8) + (lane & 7);
    const u32x4* p0 = reinterpret_cast<const u32x4*>(wv ? wg : wq) + (size_t)row0 * ld8 + col8;
    const u32x4* p1 = reinterpret_cast<const u32x4*>(wv ? wu : wq) + (size_t)row1 * ld8 + col8;
    u32x4 t0[8], t1[8];
#pragma unroll
    for (int i = 0; i < 8; ++i) t0[i] = __builtin_nontemporal_load(p0 + i * ld8);
#pragma unroll
    for (int i = 0; i < 8; ++i) t1[i] = __builtin_nontemporal_load(p1 + i * ld8);
    SB();
    const u32x4 n0 = layernorm8(xr0, lane), n1 = layernorm8(xr1, lane);
    const u32 tag = __builtin_amdgcn_readfirstlane(epoch) + 0x9E3779B9u;
    float pa[8], pb[8], r0, r1;
#pragma unroll
    for (int j = 0; j < 8; ++j) { pa[j] = 0.f; pb[j] = 0.f; }
    gu64* po = wv ? G + G_F + ((g * 2 + half) * 8 + s) * 128 + l32 : G + G_Q + ((h * 2 + half) * 4 + qk) * 256 + qc * 64 + l32;
#pragma unroll 1
    for (int blk = 0; blk < 2; ++blk) {
      const int src = ((blk ? row1 : row0) >> 3) * 4;
      u32 xa[4], xb[4];
#pragma unroll
      for (int i = 0; i < 4; ++i) {
        const u32 ta = __builtin_amdgcn_ds_bpermute(src, n0[i]), tb = __builtin_amdgcn_ds_bpermute(src, n1[i]);
        xa[i] = rg < 7 ? ta : 0u; xb[i] = rg < 7 ? tb : 0u;
      }
      gemv8(t0, xa, xb, pa, pb);
#pragma unroll
      for (int i = 0; i < 8; ++i) t0[i] = t1[i];
      if (wv || blk) {
        reduce_rg(pa, pb, lane, r0, r1);
        put(po, tag, __float_as_uint(r0)); put(po + 32, tag, __float_as_uint(r1));
        po += 64;
#pragma unroll
        for (int j = 0; j < 8; ++j) { pa[j] = 0.f; pb[j] = 0.f; }
      }
    }
    if (wv) return;
    if (lane == 0) put(G + G_T + h * 16 + j16, tag, tstart);
    __builtin_amdgcn_s_sleep(16);
    {
      gu64* qg = G + G_Q + (h * 2 + b) * 1024 + half * 128 + l32;
      float qs[4] = {0.f, 0.f, 0.f, 0.f}; u32 tv = tstart;
#pragma unroll 1
      for (unsigned spin = 0; spin < SPIN_CAP; ++spin) {
        const u64 tt = get(G + G_T + h * 16 + (lane & 15));
        tv = (u32)tt;
        if (!__all((u32)(tt >> 32) == tag)) { __builtin_amdgcn_s_sleep(4); continue; }
        u64 v[16];
#pragma unroll
        for (int k = 0; k < 4; ++k)
#pragma unroll
          for (int j = 0; j < 4; ++j) v[k * 4 + j] = get(qg + k * 256 + j * 32);
        bool ok = true;
#pragma unroll
        for (int k = 0; k < 16; ++k) ok &= (u32)(v[k] >> 32) == tag;
#pragma unroll
        for (int j = 0; j < 4; ++j) qs[j] = (__uint_as_float((u32)v[j]) + __uint_as_float((u32)v[4 + j])) + (__uint_as_float((u32)v[8 + j]) + __uint_as_float((u32)v[12 + j]));
        if (__all(ok)) break;
      }
      const int pr = (l32 & 7) * 4 + ((l32 >> 4) & 1) * 2 + ((l32 >> 3) & 1);
      reinterpret_cast<u32*>(lds + L_Q)[(half * 2) * 32 + pr] = cvtpk(qs[0], qs[1]);
      reinterpret_cast<u32*>(lds + L_Q)[(half * 2 + 1) * 32 + pr] = cvtpk(qs[2], qs[3]);
      const float early = -max16((float)(int)(tstart - tv));
      if (lane == 0) { reinterpret_cast<u32*>(lds)[L_TAG] = tag; reinterpret_cast<u32*>(lds)[L_TAG + 1] = tstart + (u32)(int)early; }
    }
    wg_barrier();
    return;
  }

  const int w = wv - 2, tid = threadIdx.x - 128;
  u32x4 buf[WIN];
  const GAS char* lp = (const GAS char*)Kc + (size_t)slot * (1024 * 512) + w * 1024;
  u32 loff = lane * 16;
  long long st_n = 8192, st_3 = 8192, st_5 = 8192;
  int lag = 0; u32 tref = 0;
  auto pace = [&](int n) {
    lag = __builtin_amdgcn_readfirstlane((int)((u32)__builtin_amdgcn_s_memrealtime() - tref) - (FB_OFF + n * FB_TOT / 128));
  };
  auto issue = [&](auto jc) {
    constexpr int j = decltype(jc)::value;
    asm volatile("s_waitcnt vmcnt(%1)\n\ts_cmpk_gt_i32 %0, %4\n\ts_cbranch_scc1 1f\n\ts_waitcnt vmcnt(%2)\n\ts_cmpk_lt_i32 %0, %5\n\ts_cbranch_scc0 1f\n\ts_waitcnt vmcnt(%3)\n1:"
                 :: "s"(lag), "i"(IF_HI - 1), "i"(IF_MID - 1), "i"(IF_LO - 1), "i"(FB_BAND), "i"(-FB_BAND) : "memory", "scc");
    asm volatile("" : "+s"(lp));
    buf[j] = __builtin_nontemporal_load((const GAS u32x4*)(lp + loff));
    lp += j == 3 ? st_3 : (j >= 5 && j < RING) ? st_5 : st_n;
  };
  static_for<0, WIN>([&](auto jc) {
    constexpr int j = decltype(jc)::value;
    asm volatile("s_waitcnt vmcnt(%0)" :: "i"(IF_MID - 1) : "memory");
    asm volatile("" : "+s"(lp));
    buf[j] = __builtin_nontemporal_load((const GAS u32x4*)(lp + loff));
    lp += 8192;
  });
  SB();
  wg_barrier();
  const u32x4 qp = *reinterpret_cast<const u32x4*>(lds + L_Q + l32 * 4);
  const u32 tag = __builtin_amdgcn_readfirstlane(reinterpret_cast<const u32*>(lds)[L_TAG]);
  tref = __builtin_amdgcn_readfirstlane(reinterpret_cast<const u32*>(lds)[L_TAG + 1]);

  float sr = 0.f, s0 = 0.f;
  pace(WIN);
  static_for<0, WIN>([&](auto jc) {
    constexpr int j = decltype(jc)::value;
    const u32x4 kv = buf[j];
    float sacc = dot2(kv[0], qp[0], 0.f);
    sacc = dot2(kv[1], qp[1], sacc); sacc = dot2(kv[2], qp[2], sacc); sacc = dot2(kv[3], qp[3], sacc);
    asm volatile("" : "+v"(sacc));
    if constexpr ((j & 3) == 0) issue(IC<j / 4>{});
    sacc = sum32(sacc);
    s0 = (l32 == j) ? sacc : s0;
    asm volatile("" : "+v"(s0));
    SB();
  });
#pragma unroll 1
  for (int it = 0; it < 4; ++it) {
    if (it == 3) lp = (const GAS char*)Vc + (size_t)slot * (1024 * 512) + w * 1024;
    pace(40 + it * 8);
    const int lj = l32 - it * 8;
    static_for<0, RING>([&](auto jc) {
      constexpr int j = decltype(jc)::value;
      const u32x4 kv = buf[j];
      float sacc = dot2(kv[0], qp[0], 0.f);
      sacc = dot2(kv[1], qp[1], sacc); sacc = dot2(kv[2], qp[2], sacc); sacc = dot2(kv[3], qp[3], sacc);
      asm volatile("" : "+v"(sacc));
      issue(jc);
      sacc = sum32(sacc);
      sr = (lj == j) ? sacc : sr;
      SB();
    });
  }
  float* const pw = lds + L_P + (w * 2 + half) * NLD;
  {
    const float a = rbf(s0) * C2, c = rbf(sr) * C2;
    const float mx = max64(fmaxf(a, c));
    const float p0 = __builtin_amdgcn_exp2f(a - mx), p1 = __builtin_amdgcn_exp2f(c - mx);
    pw[l32] = p0; pw[32 + l32] = p1;
    const float ls = sum64(p0 + p1);
    if (lane == 0) { lds[L_ML + 2 * w] = mx; lds[L_ML + 2 * w + 1] = ls; }
  }
  u16 xv = 0;
  if (slot < NCONS && tid < CCOL) xv = x[slot * CCOL + tid];
  lds_wait();
  SB();

  const int ib = h * 8 + s;
  f32x2 ac[4];
#pragma unroll
  for (int j = 0; j < 4; ++j) ac[j] = f32x2{0.f, 0.f};
#pragma unroll 1
  for (int it = 0; it < 8; ++it) {
    pace(72 + it * 8);
    if (it == 7) {
      lp = (const GAS char*)wo + (size_t)(h * DH + s * 32 + 4 * w) * (D * 2);
      const GAS char* wdp = (const GAS char*)wd + (size_t)((ib < 112 ? ib : 0) * 16 + 2 * w) * (D * 2);
      loff = l56 * 16; st_n = D * 2; st_3 = wdp - (lp + 3 * D * 2); st_5 = 0; lag = 10000;
    }
    f32x4 pr;
    static_for<0, RING>([&](auto jc) {
      constexpr int j = decltype(jc)::value;
      if constexpr ((j & 3) == 0) pr = *reinterpret_cast<const f32x4*>(pw + it * 8 + j);
      const u32x4 vv = buf[j];
      const float p = pr[j & 3];
#pragma unroll
      for (int k = 0; k < 4; ++k) ac[k] = __builtin_elementwise_fma(f32x2{p, p}, f32x2{bflo(vv[k]), bfhi(vv[k])}, ac[k]);
      asm volatile("" : "+v"(ac[0]), "+v"(ac[1]), "+v"(ac[2]), "+v"(ac[3]));
      issue(jc);
      SB();
    });
  }
  ST(9);

  {
    float a[8];
#pragma unroll
    for (int j = 0; j < 4; ++j) { a[2 * j] = x32sum(ac[j][0]); a[2 * j + 1] = x32sum(ac[j][1]); }
    *reinterpret_cast<f32x4*>(lds + L_O + w * 256 + l32 * 8 + half * 4) = half ? f32x4{a[4], a[5], a[6], a[7]} : f32x4{a[0], a[1], a[2], a[3]};
    wg_barrier();
    if (tid < 256) {
      float M = -INFINITY, L = 0.f, o = 0.f;
#pragma unroll
      for (int ww = 0; ww < NW; ++ww) M = fmaxf(M, lds[L_ML + 2 * ww]);
#pragma unroll
      for (int ww = 0; ww < NW; ++ww) {
        const float sc = __builtin_amdgcn_exp2f(lds[L_ML + 2 * ww] - M);
        L = fmaf(sc, lds[L_ML + 2 * ww + 1], L);
        o = fmaf(sc, lds[L_O + ww * 256 + tid], o);
      }
      put(G + G_A + slot * 256 + tid, tag, __float_as_uint(o));
      if (tid < 2) put(G + G_ST + slot * 2 + tid, tag, __float_as_uint(tid ? L : M));
      if (w == 3) {
        float hv = 0.f;
        if (ib < 112) {
          const int col = (ib & 3) * 16 + (lane & 15);
          const int pos = (col & 1) * 32 + (col >> 3) + ((col >> 2) & 1) * 16 + ((col >> 1) & 1) * 8;
          gu64* fg = G + G_F + ((((ib >> 2) * 2 + b) * 8 + half * 4) * 2 + ((lane >> 4) & 1)) * 64 + pos;
          float sum = 0.f;
#pragma unroll 1
          for (unsigned spin = 0; spin < SPIN_CAP; ++spin) {
            bool ok = true; sum = 0.f;
#pragma unroll
            for (int k = 0; k < 4; ++k) { const u64 v = get(fg + k * 128); sum += __uint_as_float((u32)v); ok &= (u32)(v >> 32) == tag; }
            if (__all(ok)) break;
            __builtin_amdgcn_s_sleep(8);
          }
          sum = rbf(x32sum(sum));
          auto sw = __builtin_amdgcn_permlane16_swap(__float_as_uint(sum), __float_as_uint(sum), false, false);
          const float gv = __uint_as_float(sw[0]), uv = __uint_as_float(sw[1]);
          hv = rbf(rbf(gv / (1.f + __expf(-gv))) * uv);
        }
        if (lane < 16) lds[L_H + lane] = hv;
      }
    } else {
      const int t = tid - 256, d = t >> 3, sp = t & 7;
      gu64* pa = G + G_A + (g * 8 + sp) * 256 + s * 32 + d;
      gu64* pm = G + G_ST + (g * 8 + sp) * 2;
      float av = 0.f, mv = 0.f, lv = 0.f;
      __builtin_amdgcn_s_sleep(20);
      bool ok = false;
#pragma unroll 1
      for (unsigned spin = 0; spin < SPIN_CAP; ++spin) {
        if (!ok) {
          const u64 ta = get(pa), tm = get(pm), tl = get(pm + 1);
          av = __uint_as_float((u32)ta); mv = __uint_as_float((u32)tm); lv = __uint_as_float((u32)tl);
          ok = (u32)(ta >> 32) == tag && (u32)(tm >> 32) == tag && (u32)(tl >> 32) == tag;
        }
        if (__all(ok)) break;
        __builtin_amdgcn_s_sleep(1);
      }
      const float M = max8(mv);
      const float e = __builtin_amdgcn_exp2f(mv - M);
      const float L = sum8(e * lv), o = sum8(e * av);
      if (sp == 0) lds[L_ON + d] = o / L;
    }
#pragma unroll
    for (int k = 0; k < 3; ++k) {
      const u32x4 ra = buf[2 * k], rc = buf[2 * k + 1];
#pragma unroll
      for (int j = 0; j < 4; ++j) { buf[2 * k][j] = __builtin_amdgcn_perm(rc[j], ra[j], 0x05040100u); buf[2 * k + 1][j] = __builtin_amdgcn_perm(rc[j], ra[j], 0x07060302u); }
    }
    wg_barrier();
    const f32x2 hh = *reinterpret_cast<const f32x2*>(lds + L_H + 2 * w);
    const f32x4 ov = *reinterpret_cast<const f32x4*>(lds + L_ON + 4 * w);
    const u32 h01 = cvtpk(hh[0], hh[1]), o01 = cvtpk(ov[0], ov[1]), o23 = cvtpk(ov[2], ov[3]);
    float y[8];
#pragma unroll
    for (int j = 0; j < 4; ++j) {
      y[2 * j] = dot2(buf[2][j], o23, dot2(buf[0][j], o01, dot2(buf[4][j], h01, 0.f)));
      y[2 * j + 1] = dot2(buf[3][j], o23, dot2(buf[1][j], o01, dot2(buf[5][j], h01, 0.f)));
    }
    if (lane < 56) {
      *reinterpret_cast<f32x4*>(lds + L_YP + w * D + lane * 8) = f32x4{y[0], y[1], y[2], y[3]};
      *reinterpret_cast<f32x4*>(lds + L_YP + w * D + lane * 8 + 4) = f32x4{y[4], y[5], y[6], y[7]};
    }
    wg_barrier();
    if (tid < D) {
      float t = 0.f;
#pragma unroll
      for (int ww = 0; ww < NW; ++ww) t += lds[L_YP + ww * D + tid];
      put(G + G_Y + slot * D + tid, tag, __float_as_uint(t));
    }
  }

  if (slot < NCONS) {
    gu64* src = G + G_Y + ((slot / 28) * 128 + (tid >> 4) * 4) * D + (slot % 28) * 16 + (tid & 15);
    float v = 0.f;
    __builtin_amdgcn_s_sleep(12);
    bool ok = false;
#pragma unroll 1
    for (unsigned spin = 0; spin < SPIN_CAP; ++spin) {
      if (!ok) {
        ok = true; v = 0.f;
#pragma unroll
        for (int k = 0; k < 4; ++k) { const u64 t = get(src + k * D); v += __uint_as_float((u32)t); ok &= (u32)(t >> 32) == tag; }
      }
      if (__all(ok)) break;
      __builtin_amdgcn_s_sleep(1);
    }
    v = x32sum(x16sum(v));
    if (lane < 16) lds[L_O + w * 16 + lane] = v;
    wg_barrier();
    if (tid < CCOL) {
      float r = 0.f;
#pragma unroll
      for (int ww = 0; ww < NW; ++ww) r += lds[L_O + ww * 16 + tid];
      r += __uint_as_float((u32)xv << 16);
      out[slot * CCOL + tid] = (u16)(cvtpk(r, r) & 0xffffu);
    }
  } else if (slot == NWG - 1) {
    if (w < 4) {
      bool ok = false;
#pragma unroll 1
      for (unsigned spin = 0; spin < SPIN_CAP; ++spin) {
        if (!ok) ok = (u32)(get(G + G_Y + tid * D) >> 32) == tag;
        if (__all(ok)) break;
        __builtin_amdgcn_s_sleep(2);
      }
    }
    wg_barrier();
    if (tid == 0) __hip_atomic_store((gu32*)(G + G_E), tag, RLX_AGENT);
  }
  ST(11);
}

extern "C" void kernel_launch(void* const* d_in, const int* in_sizes, int n_in,
                              void* d_out, int out_size, void* d_ws, size_t ws_size,
                              hipStream_t stream) {
  fused_decode_k<<<NWG, NT, 0, stream>>>((const u16*)d_in[0], (const u32x4*)d_in[1], (const u32x4*)d_in[2], (const u16*)d_in[3],
                                         (const u16*)d_in[4], (const u16*)d_in[6], (const u16*)d_in[7], (const u16*)d_in[8],
                                         (u64*)d_ws, (u16*)d_out);
}
```

```cpp
#include <hip/hip_runtime.h>
#include <hip/hip_bf16.h>
#include <stdint.h>

typedef unsigned int u32;
typedef unsigned short u16;
typedef unsigned long long u64;
typedef __attribute__((ext_vector_type(4))) unsigned u32x4;
typedef __attribute__((ext_vector_type(4))) float f32x4;
typedef __attribute__((ext_vector_type(2))) float f32x2;
typedef __attribute__((ext_vector_type(2))) __bf16 bf16x2;
#define GAS __attribute__((address_space(1)))
typedef GAS u32 gu32;
typedef GAS u64 gu64;
#define RLX_AGENT __ATOMIC_RELAXED, __HIP_MEMORY_SCOPE_AGENT

constexpr int D = 448, H = 16, DH = 256, F = 1792;
constexpr int NWG = 256, NW = 8, NT = (NW + 2) * 64;
constexpr int RING = 8, WIN = 32, NLD = 64;
constexpr int IF_LO = 2, IF_MID = 4, IF_HI = 6;
constexpr int FB_OFF = 150, FB_TOT = 3630, FB_BAND = 50;
constexpr int NCONS = 56, CCOL = 16;
constexpr unsigned SPIN_CAP = 1u << 18;
constexpr float C2 = 0.0625f * 1.4426950408889634f;

constexpr int G_E = 0;
constexpr int G_T = 32;
constexpr int G_Q = G_T + 256;
constexpr int G_F = G_Q + 32 * 1024;
constexpr int G_ST = G_F + 28 * 2 * 8 * 128;
constexpr int G_A = G_ST + 32 * 16;
constexpr int G_Y = G_A + 256 * 256;

constexpr int L_Q = 0;
constexpr int L_TAG = L_Q + 128;
constexpr int L_P = L_TAG + 4;
constexpr int L_O = L_P + 1024;
constexpr int L_ML = L_O + NW * 256;
constexpr int L_ON = L_ML + 16;
constexpr int L_YP = L_ON + 32;
constexpr int L_H = L_YP + NW * D;
constexpr int L_END = L_H + 16;

__device__ __forceinline__ float bflo(u32 v) { return __uint_as_float(v << 16); }
__device__ __forceinline__ float bfhi(u32 v) { return __uint_as_float(v & 0xffff0000u); }
__device__ __forceinline__ u32 cvtpk(float lo, float hi) {
  return __builtin_bit_cast(u32, __builtin_convertvector(f32x2{lo, hi}, bf16x2));
}
__device__ __forceinline__ float rbf(float f) { return bflo(cvtpk(f, f)); }
__device__ __forceinline__ float dot2(u32 a, u32 b, float c) {
  return __builtin_amdgcn_fdot2_f32_bf16(__builtin_bit_cast(bf16x2, a), __builtin_bit_cast(bf16x2, b), c, false);
}
template <int CTRL> __device__ __forceinline__ float dpp(float x) {
  return __builtin_bit_cast(float, __builtin_amdgcn_mov_dpp(__builtin_bit_cast(int, x), CTRL, 0xf, 0xf, true));
}
constexpr int XOR1 = 0xB1, XOR2 = 0x4E, HMIR = 0x141, MIR = 0x140, ROR8 = 0x128;
__device__ __forceinline__ float sum8(float x) { x += dpp<XOR1>(x); x += dpp<XOR2>(x); x += dpp<HMIR>(x); return x; }
__device__ __forceinline__ float max8(float x) { x = fmaxf(x, dpp<XOR1>(x)); x = fmaxf(x, dpp<XOR2>(x)); x = fmaxf(x, dpp<HMIR>(x)); return x; }
__device__ __forceinline__ float sum16(float x) { x = sum8(x); return x + dpp<MIR>(x); }
__device__ __forceinline__ float max16(float x) { x = max8(x); return fmaxf(x, dpp<MIR>(x)); }
__device__ __forceinline__ float x16sum(float x) {
  auto s = __builtin_amdgcn_permlane16_swap(__float_as_uint(x), __float_as_uint(x), false, false);
  return __uint_as_float(s[0]) + __uint_as_float(s[1]);
}
__device__ __forceinline__ float x32sum(float x) {
  auto s = __builtin_amdgcn_permlane32_swap(__float_as_uint(x), __float_as_uint(x), false, false);
  return __uint_as_float(s[0]) + __uint_as_float(s[1]);
}
__device__ __forceinline__ float x16max(float x) {
  auto s = __builtin_amdgcn_permlane16_swap(__float_as_uint(x), __float_as_uint(x), false, false);
  return fmaxf(__uint_as_float(s[0]), __uint_as_float(s[1]));
}
__device__ __forceinline__ float x32max(float x) {
  auto s = __builtin_amdgcn_permlane32_swap(__float_as_uint(x), __float_as_uint(x), false, false);
  return fmaxf(__uint_as_float(s[0]), __uint_as_float(s[1]));
}
__device__ __forceinline__ float sum32(float x) { return x16sum(sum16(x)); }
__device__ __forceinline__ float sum64(float x) { return x32sum(x16sum(sum16(x))); }
__device__ __forceinline__ float max64(float x) { return x32max(x16max(max16(x))); }
#define SB() __builtin_amdgcn_sched_barrier(0)
template <int I> struct IC { static constexpr int value = I; };
template <int B, int E, typename Fn> __device__ __forceinline__ void static_for(Fn&& f) {
  if constexpr (B < E) { f(IC<B>{}); static_for<B + 1, E>(f); }
}
__device__ __forceinline__ void lds_wait() { asm volatile("s_waitcnt lgkmcnt(0)" ::: "memory"); }
__device__ __forceinline__ void wg_barrier() { lds_wait(); __builtin_amdgcn_s_barrier(); asm volatile("" ::: "memory"); }
__device__ __forceinline__ void put(gu64* g, u32 tag, u32 v) { __hip_atomic_store(g, ((u64)tag << 32) | v, RLX_AGENT); }
__device__ __forceinline__ u64 get(gu64* g) { return __hip_atomic_load(g, RLX_AGENT); }

__device__ __forceinline__ u32x4 layernorm8(const u32x4 xr, int lane) {
  float v[8];
#pragma unroll
  for (int j = 0; j < 4; ++j) { v[2 * j] = bflo(xr[j]); v[2 * j + 1] = bfhi(xr[j]); }
  float sm = ((v[0] + v[1]) + (v[2] + v[3])) + ((v[4] + v[5]) + (v[6] + v[7]));
  sm = lane < 56 ? sm : 0.f;
  const float mean = sum64(sm) * (1.0f / D);
  float sq = 0.f;
#pragma unroll
  for (int j = 0; j < 8; ++j) { const float d = v[j] - mean; sq += d * d; }
  sq = lane < 56 ? sq : 0.f;
  const float var = rbf(sum64(sq) * (1.0f / D));
  const float rden = 1.0f / rbf(sqrtf(rbf(var + 1e-5f)));
  const float mb = rbf(mean);
  u32x4 o;
#pragma unroll
  for (int j = 0; j < 4; ++j) {
    const u32 d = cvtpk(v[2 * j] - mb, v[2 * j + 1] - mb);
    o[j] = cvtpk(bflo(d) * rden, bfhi(d) * rden);
  }
  return o;
}
__device__ __forceinline__ void gemv8(const u32x4 (&t)[8], const u32 (&xa)[4], const u32 (&xb)[4], float (&pa)[8], float (&pb)[8]) {
#pragma unroll
  for (int i = 0; i < 4; ++i)
#pragma unroll
    for (int j = 0; j < 4; ++j) {
      const u32 lo = __builtin_amdgcn_perm(t[2 * i + 1][j], t[2 * i][j], 0x05040100u), hi = __builtin_amdgcn_perm(t[2 * i + 1][j], t[2 * i][j], 0x07060302u);
      pa[2 * j] = dot2(lo, xa[i], pa[2 * j]); pa[2 * j + 1] = dot2(hi, xa[i], pa[2 * j + 1]);
      pb[2 * j] = dot2(lo, xb[i], pb[2 * j]); pb[2 * j + 1] = dot2(hi, xb[i], pb[2 * j + 1]);
    }
}
__device__ __forceinline__ void reduce_rg(float (&pa)[8], float (&pb)[8], int lane, float& r0, float& r1) {
#pragma unroll
  for (int j = 0; j < 8; ++j) {
    auto t = __builtin_amdgcn_permlane32_swap(__float_as_uint(pa[j]), __float_as_uint(pb[j]), false, false);
    pa[j] = __uint_as_float(t[0]) + __uint_as_float(t[1]);
  }
#pragma unroll
  for (int j = 0; j < 4; ++j) {
    auto t = __builtin_amdgcn_permlane16_swap(__float_as_uint(pa[j]), __float_as_uint(pa[j + 4]), false, false);
    pa[j] = __uint_as_float(t[0]) + __uint_as_float(t[1]);
  }
  float a0 = pa[0] + dpp<ROR8>(pa[0]), a1 = pa[1] + dpp<ROR8>(pa[1]), a2 = pa[2] + dpp<ROR8>(pa[2]), a3 = pa[3] + dpp<ROR8>(pa[3]);
  asm volatile("" : "+v"(a0), "+v"(a1), "+v"(a2), "+v"(a3));
  const bool bit3 = (lane >> 3) & 1;
  r0 = bit3 ? a2 : a0; r1 = bit3 ? a3 : a1;
}

__global__ void __launch_bounds__(NT, 3) fused_decode_k(const u16* __restrict__ x, const u32x4* __restrict__ Kc, const u32x4* __restrict__ Vc,
                                                       const u16* __restrict__ wq, const u16* __restrict__ wo, const u16* __restrict__ wg,
                                                       const u16* __restrict__ wu, const u16* __restrict__ wd, u64* ws, u16* __restrict__ out) {
  __shared__ __attribute__((aligned(16))) float lds[L_END];
  gu64* const G = (gu64*)ws;
  const int lane = threadIdx.x & 63, half = lane >> 5, l32 = lane & 31;
  const int wv = __builtin_amdgcn_readfirstlane(threadIdx.x >> 6);
  const int slot = blockIdx.x;
  const int g = slot >> 3, s = slot & 7, b = g >> 4, h = g & 15;
  const int rg = lane >> 3, l56 = lane < 56 ? lane : 55;

  if (wv < 2) {
    if (wv == 1 && g >= 28) return;
    const u32 tstart = (u32)__builtin_amdgcn_s_memrealtime();
    __builtin_amdgcn_s_setprio(3);
    const int j16 = b * 8 + s, qc = j16 & 3, qk = j16 >> 2;
    const u32 epoch = __hip_atomic_load((gu32*)(G + G_E), RLX_AGENT);
    const u32x4 xr0 = __builtin_nontemporal_load(reinterpret_cast<const u32x4*>(x + l56 * 8));
    const u32x4 xr1 = __builtin_nontemporal_load(reinterpret_cast<const u32x4*>(x + D + l56 * 8));
    const int rr = rg < 7 ? rg : 6;
    const int row0 = (wv ? s * 56 : qk * 112) + rr * 8, row1 = wv ? row0 : row0 + 56;
    const int ld8 = wv ? F / 8 : H * DH / 8, col8 = (wv ? g * 8 : h * 32 + qc * 8) + (lane & 7);
    const u32x4* p0 = reinterpret_cast<const u32x4*>(wv ? wg : wq) + (size_t)row0 * ld8 + col8;
    const u32x4* p1 = reinterpret_cast<const u32x4*>(wv ? wu : wq) + (size_t)row1 * ld8 + col8;
    u32x4 t0[8], t1[8];
#pragma unroll
    for (int i = 0; i < 8; ++i) t0[i] = __builtin_nontemporal_load(p0 + i * ld8);
#pragma unroll
    for (int i = 0; i < 8; ++i) t1[i] = __builtin_nontemporal_load(p1 + i * ld8);
    SB();
    const u32x4 n0 = layernorm8(xr0, lane), n1 = layernorm8(xr1, lane);
    const u32 tag = __builtin_amdgcn_readfirstlane(epoch) + 0x9E3779B9u;
    float pa[8], pb[8], r0, r1;
#pragma unroll
    for (int j = 0; j < 8; ++j) { pa[j] = 0.f; pb[j] = 0.f; }
    gu64* po = wv ? G + G_F + ((g * 2 + half) * 8 + s) * 128 + l32 : G + G_Q + ((h * 2 + half) * 4 + qk) * 256 + qc * 64 + l32;
#pragma unroll 1
    for (int blk = 0; blk < 2; ++blk) {
      const int src = ((blk ? row1 : row0) >> 3) * 4;
      u32 xa[4], xb[4];
#pragma unroll
      for (int i = 0; i < 4; ++i) {
        const u32 ta = __builtin_amdgcn_ds_bpermute(src, n0[i]), tb = __builtin_amdgcn_ds_bpermute(src, n1[i]);
        xa[i] = rg < 7 ? ta : 0u; xb[i] = rg < 7 ? tb : 0u;
      }
      gemv8(t0, xa, xb, pa, pb);
#pragma unroll
      for (int i = 0; i < 8; ++i) t0[i] = t1[i];
      if (wv || blk) {
        reduce_rg(pa, pb, lane, r0, r1);
        put(po, tag, __float_as_uint(r0)); put(po + 32, tag, __float_as_uint(r1));
        po += 64;
#pragma unroll
        for (int j = 0; j < 8; ++j) { pa[j] = 0.f; pb[j] = 0.f; }
      }
    }
    if (wv) return;
    if (lane == 0) put(G + G_T + h * 16 + j16, tag, tstart);
    __builtin_amdgcn_s_sleep(16);
    {
      gu64* qg = G + G_Q + (h * 2 + b) * 1024 + half * 128 + l32;
      float qs[4] = {0.f, 0.f, 0.f, 0.f}; u32 tv = tstart;
#pragma unroll 1
      for (unsigned spin = 0; spin < SPIN_CAP; ++spin) {
        const u64 tt = get(G + G_T + h * 16 + (lane & 15));
        tv = (u32)tt;
        if (!__all((u32)(tt >> 32) == tag)) { __builtin_amdgcn_s_sleep(4); continue; }
        u64 v[16];
#pragma unroll
        for (int k = 0; k < 4; ++k)
#pragma unroll
          for (int j = 0; j < 4; ++j) v[k * 4 + j] = get(qg + k * 256 + j * 32);
        bool ok = true;
#pragma unroll
        for (int k = 0; k < 16; ++k) ok &= (u32)(v[k] >> 32) == tag;
#pragma unroll
        for (int j = 0; j < 4; ++j) qs[j] = (__uint_as_float((u32)v[j]) + __uint_as_float((u32)v[4 + j])) + (__uint_as_float((u32)v[8 + j]) + __uint_as_float((u32)v[12 + j]));
        if (__all(ok)) break;
      }
      const int pr = (l32 & 7) * 4 + ((l32 >> 4) & 1) * 2 + ((l32 >> 3) & 1);
      reinterpret_cast<u32*>(lds + L_Q)[(half * 2) * 32 + pr] = cvtpk(qs[0], qs[1]);
      reinterpret_cast<u32*>(lds + L_Q)[(half * 2 + 1) * 32 + pr] = cvtpk(qs[2], qs[3]);
      const float early = -max16((float)(int)(tstart - tv));
      if (lane == 0) { reinterpret_cast<u32*>(lds)[L_TAG] = tag; reinterpret_cast<u32*>(lds)[L_TAG + 1] = tstart + (u32)(int)early; }
    }
    wg_barrier();
    return;
  }

  const int w = wv - 2, tid = threadIdx.x - 128;
  u32x4 buf[WIN];
  const GAS char* lp = (const GAS char*)Kc + (size_t)slot * (1024 * 512) + w * 1024;
  u32 loff = lane * 16;
  long long st_n = 8192, st_3 = 8192, st_5 = 8192;
  int lag = 0; u32 tref = 0;
  auto pace = [&](int n) {
    lag = __builtin_amdgcn_readfirstlane((int)((u32)__builtin_amdgcn_s_memrealtime() - tref) - (FB_OFF + n * FB_TOT / 128));
  };
  auto issue = [&](auto jc) {
    constexpr int j = decltype(jc)::value;
    asm volatile("s_waitcnt vmcnt(%1)\n\ts_cmpk_gt_i32 %0, %4\n\ts_cbranch_scc1 1f\n\ts_waitcnt vmcnt(%2)\n\ts_cmpk_lt_i32 %0, %5\n\ts_cbranch_scc0 1f\n\ts_waitcnt vmcnt(%3)\n1:"
                 :: "s"(lag), "i"(IF_HI - 1), "i"(IF_MID - 1), "i"(IF_LO - 1), "i"(FB_BAND), "i"(-FB_BAND) : "memory", "scc");
    asm volatile("" : "+s"(lp));
    buf[j] = __builtin_nontemporal_load((const GAS u32x4*)(lp + loff));
    lp += j == 3 ? st_3 : (j >= 5 && j < RING) ? st_5 : st_n;
  };
  static_for<0, WIN>([&](auto jc) {
    constexpr int j = decltype(jc)::value;
    asm volatile("s_waitcnt vmcnt(%0)" :: "i"(IF_MID - 1) : "memory");
    asm volatile("" : "+s"(lp));
    buf[j] = __builtin_nontemporal_load((const GAS u32x4*)(lp + loff));
    lp += 8192;
  });
  SB();
  wg_barrier();
  const u32x4 qp = *reinterpret_cast<const u32x4*>(lds + L_Q + l32 * 4);
  const u32 tag = __builtin_amdgcn_readfirstlane(reinterpret_cast<const u32*>(lds)[L_TAG]);
  tref = __builtin_amdgcn_readfirstlane(reinterpret_cast<const u32*>(lds)[L_TAG + 1]);

  float sr = 0.f, s0 = 0.f;
  pace(WIN);
  static_for<0, WIN>([&](auto jc) {
    constexpr int j = decltype(jc)::value;
    const u32x4 kv = buf[j];
    float sacc = dot2(kv[0], qp[0], 0.f);
    sacc = dot2(kv[1], qp[1], sacc); sacc = dot2(kv[2], qp[2], sacc); sacc = dot2(kv[3], qp[3], sacc);
    asm volatile("" : "+v"(sacc));
    if constexpr ((j & 3) == 0) issue(IC<j / 4>{});
    sacc = sum32(sacc);
    s0 = (l32 == j) ? sacc : s0;
    asm volatile("" : "+v"(s0));
    SB();
  });
#pragma unroll 1
  for (int it = 0; it < 4; ++it) {
    if (it == 3) lp = (const GAS char*)Vc + (size_t)slot * (1024 * 512) + w * 1024;
    pace(40 + it * 8);
    const int lj = l32 - it * 8;
    static_for<0, RING>([&](auto jc) {
      constexpr int j = decltype(jc)::value;
      const u32x4 kv = buf[j];
      float sacc = dot2(kv[0], qp[0], 0.f);
      sacc = dot2(kv[1], qp[1], sacc); sacc = dot2(kv[2], qp[2], sacc); sacc = dot2(kv[3], qp[3], sacc);
      asm volatile("" : "+v"(sacc));
      issue(jc);
      sacc = sum32(sacc);
      sr = (lj == j) ? sacc : sr;
      SB();
    });
  }
  float* const pw = lds + L_P + (w * 2 + half) * NLD;
  {
    const float a = rbf(s0) * C2, c = rbf(sr) * C2;
    const float mx = max64(fmaxf(a, c));
    const float p0 = __builtin_amdgcn_exp2f(a - mx), p1 = __builtin_amdgcn_exp2f(c - mx);
    pw[l32] = p0; pw[32 + l32] = p1;
    const float ls = sum64(p0 + p1);
    if (lane == 0) { lds[L_ML + 2 * w] = mx; lds[L_ML + 2 * w + 1] = ls; }
  }
  u16 xv = 0;
  if (slot < NCONS && tid < CCOL) xv = x[slot * CCOL + tid];
  lds_wait();
  SB();

  const int ib = h * 8 + s;
  f32x2 ac[4];
#pragma unroll
  for (int j = 0; j < 4; ++j) ac[j] = f32x2{0.f, 0.f};
#pragma unroll 1
  for (int it = 0; it < 8; ++it) {
    pace(72 + it * 8);
    if (it == 7) {
      lp = (const GAS char*)wo + (size_t)(h * DH + s * 32 + 4 * w) * (D * 2);
      const GAS char* wdp = (const GAS char*)wd + (size_t)((ib < 112 ? ib : 0) * 16 + 2 * w) * (D * 2);
      loff = l56 * 16; st_n = D * 2; st_3 = wdp - (lp + 3 * D * 2); st_5 = 0; lag = 10000;
    }
    f32x4 pr;
    static_for<0, RING>([&](auto jc) {
      constexpr int j = decltype(jc)::value;
      if constexpr ((j & 3) == 0) pr = *reinterpret_cast<const f32x4*>(pw + it * 8 + j);
      const u32x4 vv = buf[j];
      const float p = pr[j & 3];
#pragma unroll
      for (int k = 0; k < 4; ++k) ac[k] = __builtin_elementwise_fma(f32x2{p, p}, f32x2{bflo(vv[k]), bfhi(vv[k])}, ac[k]);
      asm volatile("" : "+v"(ac[0]), "+v"(ac[1]), "+v"(ac[2]), "+v"(ac[3]));
      issue(jc);
      SB();
    });
  }

  {
    float a[8];
#pragma unroll
    for (int j = 0; j < 4; ++j) { a[2 * j] = x32sum(ac[j][0]); a[2 * j + 1] = x32sum(ac[j][1]); }
    *reinterpret_cast<f32x4*>(lds + L_O + w * 256 + l32 * 8 + half * 4) = half ? f32x4{a[4], a[5], a[6], a[7]} : f32x4{a[0], a[1], a[2], a[3]};
    wg_barrier();
    if (tid < 256) {
      float M = -INFINITY, L = 0.f, o = 0.f;
#pragma unroll
      for (int ww = 0; ww < NW; ++ww) M = fmaxf(M, lds[L_ML + 2 * ww]);
#pragma unroll
      for (int ww = 0; ww < NW; ++ww) {
        const float sc = __builtin_amdgcn_exp2f(lds[L_ML + 2 * ww] - M);
        L = fmaf(sc, lds[L_ML + 2 * ww + 1], L);
        o = fmaf(sc, lds[L_O + ww * 256 + tid], o);
      }
      put(G + G_A + slot * 256 + tid, tag, __float_as_uint(o));
      if (tid < 2) put(G + G_ST + slot * 2 + tid, tag, __float_as_uint(tid ? L : M));
      if (w == 3) {
        float hv = 0.f;
        if (ib < 112) {
          const int col = (ib & 3) * 16 + (lane & 15);
          const int pos = (col & 1) * 32 + (col >> 3) + ((col >> 2) & 1) * 16 + ((col >> 1) & 1) * 8;
          gu64* fg = G + G_F + ((((ib >> 2) * 2 + b) * 8 + half * 4) * 2 + ((lane >> 4) & 1)) * 64 + pos;
          float sum = 0.f;
#pragma unroll 1
          for (unsigned spin = 0; spin < SPIN_CAP; ++spin) {
            bool ok = true; sum = 0.f;
#pragma unroll
            for (int k = 0; k < 4; ++k) { const u64 v = get(fg + k * 128); sum += __uint_as_float((u32)v); ok &= (u32)(v >> 32) == tag; }
            if (__all(ok)) break;
            __builtin_amdgcn_s_sleep(8);
          }
          sum = rbf(x32sum(sum));
          auto sw = __builtin_amdgcn_permlane16_swap(__float_as_uint(sum), __float_as_uint(sum), false, false);
          const float gv = __uint_as_float(sw[0]), uv = __uint_as_float(sw[1]);
          hv = rbf(rbf(gv / (1.f + __expf(-gv))) * uv);
        }
        if (lane < 16) lds[L_H + lane] = hv;
      }
    } else {
      const int t = tid - 256, d = t >> 3, sp = t & 7;
      gu64* pa = G + G_A + (g * 8 + sp) * 256 + s * 32 + d;
      gu64* pm = G + G_ST + (g * 8 + sp) * 2;
      float av = 0.f, mv = 0.f, lv = 0.f;
      __builtin_amdgcn_s_sleep(20);
      bool ok = false;
#pragma unroll 1
      for (unsigned spin = 0; spin < SPIN_CAP; ++spin) {
        if (!ok) {
          const u64 ta = get(pa), tm = get(pm), tl = get(pm + 1);
          av = __uint_as_float((u32)ta); mv = __uint_as_float((u32)tm); lv = __uint_as_float((u32)tl);
          ok = (u32)(ta >> 32) == tag && (u32)(tm >> 32) == tag && (u32)(tl >> 32) == tag;
        }
        if (__all(ok)) break;
        __builtin_amdgcn_s_sleep(1);
      }
      const float M = max8(mv);
      const float e = __builtin_amdgcn_exp2f(mv - M);
      const float L = sum8(e * lv), o = sum8(e * av);
      if (sp == 0) lds[L_ON + d] = o / L;
    }
#pragma unroll
    for (int k = 0; k < 3; ++k) {
      const u32x4 ra = buf[2 * k], rc = buf[2 * k + 1];
#pragma unroll
      for (int j = 0; j < 4; ++j) { buf[2 * k][j] = __builtin_amdgcn_perm(rc[j], ra[j], 0x05040100u); buf[2 * k + 1][j] = __builtin_amdgcn_perm(rc[j], ra[j], 0x07060302u); }
    }
    wg_barrier();
    const f32x2 hh = *reinterpret_cast<const f32x2*>(lds + L_H + 2 * w);
    const f32x4 ov = *reinterpret_cast<const f32x4*>(lds + L_ON + 4 * w);
    const u32 h01 = cvtpk(hh[0], hh[1]), o01 = cvtpk(ov[0], ov[1]), o23 = cvtpk(ov[2], ov[3]);
    float y[8];
#pragma unroll
    for (int j = 0; j < 4; ++j) {
      y[2 * j] = dot2(buf[2][j], o23, dot2(buf[0][j], o01, dot2(buf[4][j], h01, 0.f)));
      y[2 * j + 1] = dot2(buf[3][j], o23, dot2(buf[1][j], o01, dot2(buf[5][j], h01, 0.f)));
    }
    if (lane < 56) {
      *reinterpret_cast<f32x4*>(lds + L_YP + w * D + lane * 8) = f32x4{y[0], y[1], y[2], y[3]};
      *reinterpret_cast<f32x4*>(lds + L_YP + w * D + lane * 8 + 4) = f32x4{y[4], y[5], y[6], y[7]};
    }
    wg_barrier();
    if (tid < D) {
      float t = 0.f;
#pragma unroll
      for (int ww = 0; ww < NW; ++ww) t += lds[L_YP + ww * D + tid];
      put(G + G_Y + slot * D + tid, tag, __float_as_uint(t));
    }
  }

  if (slot < NCONS) {
    gu64* src = G + G_Y + ((slot / 28) * 128 + (tid >> 4) * 4) * D + (slot % 28) * 16 + (tid & 15);
    float v = 0.f;
    __builtin_amdgcn_s_sleep(12);
    bool ok = false;
#pragma unroll 1
    for (unsigned spin = 0; spin < SPIN_CAP; ++spin) {
      if (!ok) {
        ok = true; v = 0.f;
#pragma unroll
        for (int k = 0; k < 4; ++k) { const u64 t = get(src + k * D); v += __uint_as_float((u32)t); ok &= (u32)(t >> 32) == tag; }
      }
      if (__all(ok)) break;
      __builtin_amdgcn_s_sleep(1);
    }
    v = x32sum(x16sum(v));
    if (lane < 16) lds[L_O + w * 16 + lane] = v;
    wg_barrier();
    if (tid < CCOL) {
      float r = 0.f;
#pragma unroll
      for (int ww = 0; ww < NW; ++ww) r += lds[L_O + ww * 16 + tid];
      r += __uint_as_float((u32)xv << 16);
      out[slot * CCOL + tid] = (u16)(cvtpk(r, r) & 0xffffu);
    }
  } else if (slot == NWG - 1) {
    if (w < 4) {
      bool ok = false;
#pragma unroll 1
      for (unsigned spin = 0; spin < SPIN_CAP; ++spin) {
        if (!ok) ok = (u32)(get(G + G_Y + tid * D) >> 32) == tag;
        if (__all(ok)) break;
        __builtin_amdgcn_s_sleep(2);
      }
    }
    wg_barrier();
    if (tid == 0) __hip_atomic_store((gu32*)(G + G_E), tag, RLX_AGENT);
  }
}

extern "C" void kernel_launch(void* const* d_in, const int* in_sizes, int n_in,
                              void* d_out, int out_size, void* d_ws, size_t ws_size,
                              hipStream_t stream) {
  fused_decode_k<<<NWG, NT, 0, stream>>>((const u16*)d_in[0], (const u32x4*)d_in[1], (const u32x4*)d_in[2], (const u16*)d_in[3],
                                         (const u16*)d_in[4], (const u16*)d_in[6], (const u16*)d_in[7], (const u16*)d_in[8],
                                         (u64*)d_ws, (u16*)d_out);
}
```
